```python
import torch

def _softmax(x, axis=-1):
    return torch.softmax(x, dim=axis)

def _gelu(x):
    return torch.nn.functional.gelu(x, approximate='tanh')

def _silu(x):
    return torch.nn.functional.silu(x)

def _sigmoid(x):
    return torch.sigmoid(x)

def _layernorm(x, axis=-1, eps=1e-05):
    m = x.mean(dim=axis, keepdim=True)
    v = x.var(dim=axis, keepdim=True, unbiased=False)
    return (x - m) / torch.sqrt(v + eps)

def _scatter_add(tgt, idx, src, axis=0):
    return tgt.clone().index_add_(axis, idx, src)

def _segment_sum(src, seg, n):
    out = torch.zeros((n,) + src.shape[1:], dtype=src.dtype)
    return out.index_add_(0, seg, src)

def _pad(x, pw):
    flat = tuple((v for pair in reversed(pw) for v in pair))
    return torch.nn.functional.pad(x, flat)
kernel_name = 'recurrent_scan__memory_aug_recurrent_mem_tr__mixer_channel_mlp__prompt'

def setup_inputs():

    _w = lambda a: (lambda t: t.to(torch.bfloat16) if t.is_floating_point() else t)(torch.as_tensor(a))

    def _A_setup():
        B, S, Dm = (1, 2048, 640)
        H, Dh = (32, 192)
        M, seg = (8, 32)
        seg = min(seg, S)
        S = S // seg * seg
        return {'x': _w((lambda *s: torch.randn(*s, dtype=torch.bfloat16) * 0.05)(B, S, Dm)), 'mem0': _w((lambda *s: torch.randn(*s, dtype=torch.bfloat16) * 0.8)(B, M, Dm)), 'Wq': _w((lambda *s: torch.randn(*s, dtype=torch.bfloat16) * 0.8)(Dm, H, Dh) * Dm ** (-0.5)), 'Wk': _w((lambda *s: torch.randn(*s, dtype=torch.bfloat16) * 0.8)(Dm, H, Dh) * Dm ** (-0.5)), 'Wv': _w((lambda *s: torch.randn(*s, dtype=torch.bfloat16) * 0.8)(Dm, H, Dh) * Dm ** (-0.5)), 'Wo': _w((lambda *s: torch.randn(*s, dtype=torch.bfloat16) * 0.8)(H, Dh, Dm) * (H * Dh) ** (-0.5)), 'seg_len': seg}
    _A_in = _A_setup()

    def _B_setup():
        B, S, D, F = (1, 2048, 640, 1536)
        return {'x': _w((lambda *s: torch.randn(*s, dtype=torch.bfloat16) * 0.05)(B, S, D)), 'w1': _w((lambda *s: torch.randn(*s, dtype=torch.bfloat16) * 1.6)(D, F) * D ** (-0.5)), 'b1': _w((lambda *s: torch.randn(*s, dtype=torch.bfloat16) * 0.4)(F)), 'w2': _w((lambda *s: torch.randn(*s, dtype=torch.bfloat16) * 0.2)(F, D) * F ** (-0.5)), 'b2': _w((lambda *s: torch.randn(*s, dtype=torch.bfloat16) * 0.2)(D))}
    _B_in = _B_setup()
    return {'A_x': _A_in['x'], 'A_mem0': _A_in['mem0'], 'A_Wq': _A_in['Wq'], 'A_Wk': _A_in['Wk'], 'A_Wv': _A_in['Wv'], 'A_Wo': _A_in['Wo'], 'A_seg_len': _A_in['seg_len'], 'B_x': _B_in['x'], 'B_w1': _B_in['w1'], 'B_b1': _B_in['b1'], 'B_w2': _B_in['w2'], 'B_b2': _B_in['b2']}

def reference(A_x, A_mem0, A_Wq, A_Wk, A_Wv, A_Wo, A_seg_len, B_x, B_w1, B_b1, B_w2, B_b2):

    def _A_ref(x, mem0, Wq, Wk, Wv, Wo, seg_len):
        B, S, Dm = x.shape
        H, Dh = (Wq.shape[1], Wq.shape[2])
        M = mem0.shape[1]
        scale = Dh ** (-0.5)
        n_seg = S // seg_len
        L = M + seg_len
        idx = torch.arange(L)
        mask = (idx[:, None] < M) | (idx[:, None] >= idx[None, :])
        mem = mem0
        outs = []
        for s in range(n_seg):
            xs = x[:, s * seg_len:(s + 1) * seg_len, :].clone()
            h = torch.concatenate([mem, xs], axis=1)
            q = torch.einsum('btd,dhe->bhte', h, Wq)
            k = torch.einsum('btd,dhe->bhte', h, Wk)
            v = torch.einsum('btd,dhe->bhte', h, Wv)
            scores = torch.einsum('bhqe,bhke->bhqk', q, k) * scale
            scores = torch.where(mask[None, None], scores, -1000000000.0)
            p = _softmax(scores, axis=-1)
            o = torch.einsum('bhqk,bhke->bhqe', p, v)
            o = torch.einsum('bhte,hed->btd', o, Wo)
            mem = o[:, :M, :].clone()
            outs.append(o[:, M:, :])
        return torch.concatenate(outs, axis=1)

    def _B_ref(x, w1, b1, w2, b2):
        y = _layernorm(x, axis=-1)
        h = _gelu(torch.einsum('bsd,df->bsf', y, w1) + b1)
        return torch.einsum('bsf,fd->bsd', h, w2) + b2
    x = A_x
    xn = _layernorm(x, axis=-1)
    s = _A_ref(xn, A_mem0, A_Wq, A_Wk, A_Wv, A_Wo, A_seg_len)
    g = _silu(_B_ref(xn, B_w1, B_b1, B_w2, B_b2))
    return x + s * g
```

```cpp
#include <hip/hip_runtime.h>
#include <hip/hip_bf16.h>
#include <stdint.h>

#define GAS __attribute__((address_space(1)))
#define LAS __attribute__((address_space(3)))
typedef unsigned short u16;
typedef unsigned v2u __attribute__((ext_vector_type(2)));
typedef unsigned v4u __attribute__((ext_vector_type(4)));
typedef unsigned v8u __attribute__((ext_vector_type(8)));
typedef float f32x4 __attribute__((ext_vector_type(4)));
typedef __bf16 bf16x2 __attribute__((ext_vector_type(2)));
typedef __bf16 bf16x8 __attribute__((ext_vector_type(8)));
typedef __bf16 bf16x16 __attribute__((ext_vector_type(16)));
typedef short s16x2 __attribute__((ext_vector_type(2)));
typedef short s16x4 __attribute__((ext_vector_type(4)));
typedef GAS unsigned gu32;
typedef LAS unsigned char lbyte;
#define RLX_AGENT __ATOMIC_RELAXED, __HIP_MEMORY_SCOPE_AGENT
#define DEV __device__ __forceinline__

#define SPLIT 0

namespace rk {
constexpr int NSEG = 64;
constexpr int CTRL_BYTES = 65536;
constexpr int R_MEM = 0, R_SC = 10240, R_ZM = 51200, RING_SLOT = 51200 + 327680;
constexpr int OFF_RING = CTRL_BYTES;
constexpr int OFF_XN = OFF_RING + 4 * RING_SLOT;
constexpr int OFF_Y = OFF_XN + 2621440;
constexpr int OFF_K = OFF_Y + 2621440;
constexpr int OFF_V = OFF_K + 25165824;
constexpr int OFF_Q = OFF_V + 25165824;
constexpr int OFF_H = OFF_Q + 25165824;
constexpr int OFF_U = OFF_H + 6291456;
constexpr int OFF_G = OFF_U + 83886080;
constexpr int OFF_ZMP = OFF_G + 20971520;
constexpr int OFF_KM = OFF_ZMP + 20971520;
constexpr int OFF_P = OFF_KM + 12582912;
constexpr int OFF_PM = OFF_P + 4194304;
constexpr int OFF_DBG = OFF_PM + 1048576;
constexpr int WS_END = OFF_DBG + 4194304;
static_assert(WS_END <= 256 * 1024 * 1024, "workspace");
constexpr int L_TMO = 0, L_START = 1, L_QUEUE = 9, L_PK = 16, L_PV = 48, L_PO = 80, L_PQ = 112, L_PH = 208, L_PG = 216,
              L_PS = 224, L_PUV = 288, L_PSC = 352;
constexpr unsigned SENT = 0xFFFFFFFFu;
constexpr unsigned SPIN_CAP = 1u << 17;
constexpr int LDS_BYTES = 155648;
constexpr float SCL2 = 0.07216878364870322f * 1.4426950408889634f;

struct Params {
  const u16 *x, *mem0, *wq, *wk, *wv, *wo, *w1, *b1, *w2, *b2;
  u16* out;
  unsigned char* ws;
  int roles;
  int dA, dB, dN;
};

DEV float bflo(unsigned u) { return __builtin_bit_cast(float, u << 16); }
DEV float bfhi(unsigned u) { return __builtin_bit_cast(float, u & 0xFFFF0000u); }
DEV unsigned pk2(float a, float b) { bf16x2 v; v[0] = (__bf16)a; v[1] = (__bf16)b; return __builtin_bit_cast(unsigned, v); }
DEV float rbf(float a) { return (float)(__bf16)a; }
DEV unsigned nosent(unsigned u) { return u < 0xFFFFFFFEu ? u : 0xFFFFFFFEu; }
DEV f32x4 mfma(v4u a, v4u b, f32x4 c) {
  return __builtin_amdgcn_mfma_f32_16x16x32_bf16(__builtin_bit_cast(bf16x8, a), __builtin_bit_cast(bf16x8, b), c, 0, 0, 0);
}
DEV f32x4 smfma(v4u a, v8u b, f32x4 c, int idx) {
  return __builtin_amdgcn_smfmac_f32_16x16x64_bf16(__builtin_bit_cast(bf16x8, a), __builtin_bit_cast(bf16x16, b), c, idx, 0, 0);
}
DEV v4u lo4(v8u w) { return __builtin_shufflevector(w, w, 0, 1, 2, 3); }
DEV v4u hi4(v8u w) { return __builtin_shufflevector(w, w, 4, 5, 6, 7); }
DEV v2u trd(lbyte* p) { return __builtin_bit_cast(v2u, __builtin_amdgcn_ds_read_tr16_b64_v4i16((LAS s16x4*)p)); }
DEV v4u lds128(lbyte* p) { return *(LAS v4u*)p; }
DEV void glds16(const void* gsrc, unsigned lds_dst) {
  unsigned keep;
  asm volatile("s_mov_b32 %0, m0\n\ts_mov_b32 m0, %2\n\ts_nop 0\n\tglobal_load_lds_dwordx4 %1, off\n\ts_mov_b32 m0, %0"
               : "=&s"(keep) : "v"(gsrc), "s"(lds_dst) : "memory");
}
#define VMWAIT(n) asm volatile("s_waitcnt vmcnt(" #n ")" ::: "memory")
#define CONV() __builtin_amdgcn_wave_barrier()
#define BAR() do { asm volatile("s_waitcnt lgkmcnt(0)" ::: "memory"); __builtin_amdgcn_s_barrier(); asm volatile("" ::: "memory"); } while (0)
typedef __amdgpu_buffer_rsrc_t rsrc_t;
DEV v4u ld16(rsrc_t rs, int off) { return __builtin_amdgcn_raw_buffer_load_b128(rs, off, 0, 16); }
DEV v2u ld8(rsrc_t rs, int off) { return __builtin_amdgcn_raw_buffer_load_b64(rs, off, 0, 16); }
DEV void st16(rsrc_t rs, int off, v4u v) { __builtin_amdgcn_raw_buffer_store_b128(v, rs, off, 0, 16); }
DEV void st8(rsrc_t rs, int off, v2u v) { __builtin_amdgcn_raw_buffer_store_b64(v, rs, off, 0, 16); }
DEV unsigned ct32() { return (unsigned)__builtin_amdgcn_s_memtime(); }
DEV void wait_until(unsigned t) { while ((int)(ct32() - t) < 0) {} }
DEV float sw32_add(float a, float b) {
  auto r = __builtin_amdgcn_permlane32_swap(__builtin_bit_cast(unsigned, a), __builtin_bit_cast(unsigned, b), false, false);
  return __builtin_bit_cast(float, (unsigned)r[0]) + __builtin_bit_cast(float, (unsigned)r[1]);
}
DEV float sw16_add(float a, float b) {
  auto r = __builtin_amdgcn_permlane16_swap(__builtin_bit_cast(unsigned, a), __builtin_bit_cast(unsigned, b), false, false);
  return __builtin_bit_cast(float, (unsigned)r[0]) + __builtin_bit_cast(float, (unsigned)r[1]);
}
DEV float x32_max(float a) {
  auto r = __builtin_amdgcn_permlane32_swap(__builtin_bit_cast(unsigned, a), __builtin_bit_cast(unsigned, a), false, false);
  return fmaxf(__builtin_bit_cast(float, (unsigned)r[0]), __builtin_bit_cast(float, (unsigned)r[1]));
}
DEV float x16_max(float a) {
  auto r = __builtin_amdgcn_permlane16_swap(__builtin_bit_cast(unsigned, a), __builtin_bit_cast(unsigned, a), false, false);
  return fmaxf(__builtin_bit_cast(float, (unsigned)r[0]), __builtin_bit_cast(float, (unsigned)r[1]));
}
template <int CTRL> DEV float dpp(float v) {
  return __builtin_bit_cast(float, __builtin_amdgcn_update_dpp(0, __builtin_bit_cast(int, v), CTRL, 0xf, 0xf, true));
}
constexpr int DPP_XOR1 = 0xB1, DPP_XOR2 = 0x4E, DPP_ROR8 = 0x128, DPP_ROR4 = 0x124;
DEV float wave_sum(float v) {
  v += dpp<DPP_XOR1>(v); v += dpp<DPP_XOR2>(v); v += dpp<DPP_ROR4>(v); v += dpp<DPP_ROR8>(v);
  v = sw16_add(v, v); v = sw32_add(v, v);
  return v;
}

struct Ctx {
  rsrc_t rs;
  gu32* ctl;
  lbyte* L;
  unsigned l0;
  int tid, wave, lane;
};
DEV bool spin_fail(unsigned& spins, gu32* ctl, unsigned code) {
  if (++spins > SPIN_CAP) { __hip_atomic_store(ctl, 1u, RLX_AGENT); __hip_atomic_fetch_max(ctl + 1, code, RLX_AGENT); return true; }
  return (spins & 63u) == 0u && __builtin_amdgcn_readfirstlane(__hip_atomic_load(ctl, RLX_AGENT)) != 0;
}
DEV void wait_lines(const Ctx& c, int line, unsigned want, unsigned code) {
  for (unsigned spins = 0;;) {
    const unsigned v = __hip_atomic_load(c.ctl + line * 32, RLX_AGENT);
    if (__all(v >= want)) break;
    __builtin_amdgcn_s_sleep(2);
    if (spin_fail(spins, c.ctl, code)) break;
  }
  __builtin_amdgcn_fence(__ATOMIC_ACQUIRE, "wavefront");
}
DEV void set_line(const Ctx& c, int line, unsigned v) { __hip_atomic_store(c.ctl + line * 32, v, RLX_AGENT); }

template <bool PERM>
DEV void load_wA(const Ctx& c, v8u (&w)[3][5], const u16* s0, const u16* s1, int ld, int split) {
  const int kh = c.wave >> 2, fg = c.wave & 3, q = c.lane >> 4, qq = (c.lane >> 2) & 3, p = c.lane & 3;
  const u16* src[6];
#pragma unroll
  for (int i = 0; i < 6; ++i) {
    const int byte = (c.wave * 6 + i) * 1024 + c.lane * 16, lr = byte / 384, cp = (byte % 384) >> 4;
    const int s = PERM ? ((lr >> 2) & 3) : (((lr >> 1) & 1) | (((lr >> 3) & 1) << 1));
    const int cs = cp ^ (s << 1);
    const int row = (lr >> 6) * 320 + (lr & 63);
    src[i] = (cs < split ? s0 + cs * 8 : s1 + (cs - split) * 8) + (size_t)row * ld;
  }
  auto issue = [&](int ch) {
#pragma unroll
    for (int i = 0; i < 6; ++i) glds16(src[i] + (size_t)ch * 64 * ld, c.l0 + (ch % 3) * 49152 + (c.wave * 6 + i) * 1024);
  };
  issue(0); issue(1); issue(2);
  const int ro = PERM ? (16 * (q >> 1) + 4 * (q & 1) + (qq & 1) + 8 * (qq >> 1)) : (8 * q + qq);
  const int sl = PERM ? ((q & 1) + 2 * (qq >> 1)) : (((qq >> 1) & 1) | ((q & 1) << 1));
  constexpr int SEC = PERM ? 2 * 384 : 4 * 384;
#pragma unroll
  for (int ch = 0; ch < 5; ++ch) {
    if (ch <= 2) VMWAIT(12); else if (ch == 3) VMWAIT(6); else VMWAIT(0);
    BAR();
    lbyte* base = c.L + (ch % 3) * 49152 + (kh * 64 + ro) * 384 + (p & 1) * 8;
#pragma unroll
    for (int t = 0; t < 3; ++t) {
      lbyte* a = base + (((2 * (3 * fg + t) + (p >> 1)) ^ (sl << 1)) << 4);
      const v2u r0 = trd(a), r1 = trd(a + SEC), r2 = trd(a + 32 * 384), r3 = trd(a + 32 * 384 + SEC);
      w[t][ch] = (v8u){r0.x, r0.y, r1.x, r1.y, r2.x, r2.y, r3.x, r3.y};
    }
    BAR();
    if (ch + 3 < 5) issue(ch + 3);
  }
}
DEV void load_wB(const Ctx& c, v4u (&w)[5][6], const u16* s0) {
  const int q = c.lane >> 4, qq = (c.lane >> 2) & 3, p = c.lane & 3;
  const u16* src[5];
#pragma unroll
  for (int i = 0; i < 5; ++i) {
    const int byte = (c.wave * 5 + i) * 1024 + c.lane * 16, lr = byte / 1280, cp = (byte % 1280) >> 4;
    const int s = (lr & 3) | (((lr >> 3) & 1) << 2);
    src[i] = s0 + (size_t)lr * 640 + (cp ^ (s << 1)) * 8;
  }
  auto issue = [&](int ch) {
#pragma unroll
    for (int i = 0; i < 5; ++i) glds16(src[i] + (size_t)ch * 32 * 640, c.l0 + (ch % 3) * 40960 + (c.wave * 5 + i) * 1024);
  };
  issue(0); issue(1); issue(2);
  const int sl = qq | ((q & 1) << 2);
#pragma unroll
  for (int ch = 0; ch < 6; ++ch) {
    if (ch <= 3) VMWAIT(10); else if (ch == 4) VMWAIT(5); else VMWAIT(0);
    BAR();
    lbyte* base = c.L + (ch % 3) * 40960 + (8 * q + qq) * 1280 + (p & 1) * 8;
#pragma unroll
    for (int t = 0; t < 5; ++t) {
      lbyte* a = base + (((2 * (5 * c.wave + t) + (p >> 1)) ^ (sl << 1)) << 4);
      const v2u r0 = trd(a), r1 = trd(a + 4 * 1280);
      w[t][ch] = (v4u){r0.x, r0.y, r1.x, r1.y};
    }
    BAR();
    if (ch + 3 < 6) issue(ch + 3);
  }
}
DEV void load_wZ(const Ctx& c, v8u (&w)[3][3], const u16* s0, int tile0) {
  const int q = c.lane >> 4, qq = (c.lane >> 2) & 3, p = c.lane & 3;
#pragma unroll
  for (int ch = 0; ch < 3; ++ch)
#pragma unroll
    for (int i = 0; i < 5; ++i) {
      const int byte = (c.wave * 5 + i) * 1024 + c.lane * 16, lr = byte / 640, cp = (byte % 640) >> 4;
      const int s = (lr >> 2) & 3;
      glds16(s0 + (size_t)(ch * 64 + lr) * 640 + (cp ^ (s << 1)) * 8, c.l0 + ch * 40960 + (c.wave * 5 + i) * 1024);
    }
  VMWAIT(0);
  BAR();
  const int ro = 16 * (q >> 1) + 4 * (q & 1) + (qq & 1) + 8 * (qq >> 1), sl = (q & 1) + 2 * (qq >> 1);
#pragma unroll
  for (int ch = 0; ch < 3; ++ch)
#pragma unroll
    for (int t = 0; t < 3; ++t) {
      const int tile = min(tile0 + t, 19);
      lbyte* a = c.L + ch * 40960 + ro * 640 + (p & 1) * 8 + (((2 * tile + (p >> 1)) ^ (sl << 1)) << 4);
      const v2u r0 = trd(a), r1 = trd(a + 2 * 640), r2 = trd(a + 32 * 640), r3 = trd(a + 34 * 640);
      w[t][ch] = (v8u){r0.x, r0.y, r1.x, r1.y, r2.x, r2.y, r3.x, r3.y};
    }
  BAR();
}

DEV void ln_row(float (&v)[16], bool two) {
  float s = 0.f;
#pragma unroll
  for (int j = 0; j < 16; ++j) s += v[j];
  const float mean = wave_sum(s) * (1.0f / 640.0f);
  float q = 0.f;
#pragma unroll
  for (int j = 0; j < 16; ++j) { const float d = (j < 8 || two) ? v[j] - mean : 0.f; q += d * d; }
  const float var = rbf(wave_sum(q) * (1.0f / 640.0f));
  const float m = rbf(mean), sd = rbf(sqrtf(rbf(var + 1e-5f)));
#pragma unroll
  for (int j = 0; j < 16; ++j) v[j] = (j < 8 || two) ? rbf(rbf(v[j] - m) / sd) : 0.f;
}
DEV void phase_ln(const Ctx& c, const Params& p) {
  const int b = blockIdx.x, row = 8 * b + c.wave;
  const bool two = c.lane < 16;
  const v4u a0 = *(const v4u*)(p.x + (size_t)row * 640 + c.lane * 8);
  v4u a1 = {0, 0, 0, 0};
  if (two) a1 = *(const v4u*)(p.x + (size_t)row * 640 + 512 + c.lane * 8);
  float v[16];
#pragma unroll
  for (int j = 0; j < 4; ++j) { v[2 * j] = bflo(a0[j]); v[2 * j + 1] = bfhi(a0[j]); v[8 + 2 * j] = bflo(a1[j]); v[9 + 2 * j] = bfhi(a1[j]); }
#pragma unroll
  for (int pass = 0; pass < 2; ++pass) {
    ln_row(v, two);
    *(LAS v4u*)(c.L + pass * 10240 + (c.lane * 8 + c.wave) * 16) = (v4u){pk2(v[0], v[1]), pk2(v[2], v[3]), pk2(v[4], v[5]), pk2(v[6], v[7])};
    if (two) *(LAS v4u*)(c.L + pass * 10240 + ((64 + c.lane) * 8 + c.wave) * 16) = (v4u){pk2(v[8], v[9]), pk2(v[10], v[11]), pk2(v[12], v[13]), pk2(v[14], v[15])};
  }
  BAR();
  const int gb = ((b >> 1) * 20 * 64 + (b & 1) * 8) * 16;
#pragma unroll
  for (int i = 0; i < 3; ++i) {
    const int pc = c.tid + i * 512;
    if (pc < 1280) {
      const int pass = pc >= 640, pp = pc - pass * 640, ch = pp >> 3;
      st16(c.rs, (pass ? OFF_Y : OFF_XN) + gb + ((ch >> 2) * 64 + (ch & 3) * 16 + (pp & 7)) * 16, lds128(c.L + pc * 16));
    }
  }
  BAR();
}
DEV void start_arrive(const Ctx& c) {
  VMWAIT(0);
  BAR();
  if (c.tid == 0) __hip_atomic_fetch_add(c.ctl + (L_START + (blockIdx.x & 7)) * 32, 1u, RLX_AGENT);
  CONV();
}
DEV void start_wait(const Ctx& c) {
  if (c.wave == 0) {
    wait_lines(c, L_START + (c.lane & 7), 32u, 0x10);
    __builtin_amdgcn_fence(__ATOMIC_ACQUIRE, "agent");
    VMWAIT(0);
  }
  BAR();
}

template <bool GELU>
DEV void stream_A(const Ctx& c, const Params& p, const v8u (&w)[3][5], int in_off, int s0, int s1, int out_off, int out_stride, int line, const u16* bias) {
  constexpr int IN = 0, EX = 81920, IM = 81920 + 49152;
  const int kh = c.wave >> 2, fg = c.wave & 3, n = c.lane & 15, rq = c.lane >> 4;
  const int ns = s1 - s0;
  const unsigned char* gin = p.ws + in_off + (size_t)s0 * 40960 + c.wave * 5120 + c.lane * 16;
  float bs[3][4];
  if (GELU) {
#pragma unroll
    for (int t = 0; t < 3; ++t) {
      const v2u bb = *(const v2u*)(bias + 48 * fg + 16 * t + 4 * rq);
      bs[t][0] = bflo(bb.x); bs[t][1] = bfhi(bb.x); bs[t][2] = bflo(bb.y); bs[t][3] = bfhi(bb.y);
    }
  }
  int imo[3];
#pragma unroll
  for (int t = 0; t < 3; ++t) { const int e = 48 * fg + 16 * t + 4 * rq; imo[t] = ((kh * 6 + (e >> 5)) * 64 + ((e >> 3) & 3) * 16 + n) * 16 + ((e >> 2) & 1) * 8; }
#pragma unroll
  for (int i = 0; i < 5; ++i) glds16(gin + i * 1024, c.l0 + IN + c.wave * 5120 + i * 1024);
  f32x4 keep[3];
#pragma unroll
  for (int t = 0; t < 3; ++t) keep[t] = (f32x4){0.f, 0.f, 0.f, 0.f};
  for (int it = 0; it < ns + 3; ++it) {
    VMWAIT(0);
    BAR();
    if (it >= 3 && c.tid == 0) set_line(c, line, (unsigned)(s0 + it - 2));
    CONV();
    if (it >= 2 && it < ns + 2) {
      lbyte* im = c.L + IM + (it & 1) * 12288;
      const int go = out_off + (s0 + it - 2) * out_stride;
      st16(c.rs, go + c.tid * 16, lds128(im + c.tid * 16));
      if (c.wave < 4) st16(c.rs, go + 8192 + c.tid * 16, lds128(im + 8192 + c.tid * 16));
    }
    if (it + 1 < ns) {
#pragma unroll
      for (int i = 0; i < 5; ++i) glds16(gin + (size_t)(it + 1) * 40960 + i * 1024, c.l0 + IN + ((it + 1) & 1) * 40960 + c.wave * 5120 + i * 1024);
    }
    if (it >= 1 && it < ns + 1) {
      lbyte* ex = c.L + EX + ((it - 1) & 1) * 24576 + (c.wave ^ 4) * 3072 + c.lane * 16;
      lbyte* im = c.L + IM + ((it - 1) & 1) * 12288;
#pragma unroll
      for (int t = 0; t < 3; ++t) {
        f32x4 v = keep[t] + *(LAS f32x4*)(ex + t * 1024);
        if (GELU) {
#pragma unroll
          for (int r = 0; r < 4; ++r) {
            const float u = rbf(rbf(v[r]) + bs[t][r]);
            const float z = u * (1.0f + 0.044715f * u * u) * (-2.0f * 0.7978845608028654f * 1.4426950408889634f);
            v[r] = u * __builtin_amdgcn_rcpf(1.0f + __builtin_amdgcn_exp2f(z));
          }
        }
        *(LAS v2u*)(im + imo[t]) = (v2u){pk2(v[0], v[1]), pk2(v[2], v[3])};
      }
    }
    if (it < ns) {
      lbyte* in = c.L + IN + (it & 1) * 40960 + kh * 10240 + c.lane * 16;
      f32x4 acc[2][3];
#pragma unroll
      for (int t = 0; t < 3; ++t) { acc[0][t] = (f32x4){0.f, 0.f, 0.f, 0.f}; acc[1][t] = acc[0][t]; }
#pragma unroll
      for (int ks = 0; ks < 10; ++ks) {
        const v4u x0 = lds128(in + ks * 1024), x1 = lds128(in + 20480 + ks * 1024);
#pragma unroll
        for (int t = 0; t < 3; ++t) {
          const v4u wf = (ks & 1) ? hi4(w[t][ks >> 1]) : lo4(w[t][ks >> 1]);
          acc[0][t] = mfma(wf, x0, acc[0][t]);
          acc[1][t] = mfma(wf, x1, acc[1][t]);
        }
      }
      lbyte* ex = c.L + EX + (it & 1) * 24576 + c.wave * 3072 + c.lane * 16;
#pragma unroll
      for (int t = 0; t < 3; ++t) {
        *(LAS f32x4*)(ex + t * 1024) = kh ? acc[0][t] : acc[1][t];
        keep[t] = kh ? acc[1][t] : acc[0][t];
      }
    }
  }
}

DEV void stream_B(const Ctx& c, const v4u (&w)[5][6], int in_off, int in_stride, int in_line, int out_off, int out_stride, int line) {
  v4u r0, r1;
  wait_lines(c, in_line, 1u, 0x20);
  r0 = ld16(c.rs, in_off + c.tid * 16);
  r1 = ld16(c.rs, in_off + 8192 + (c.tid & 255) * 16);
  unsigned seen = 1;
  for (int it = 0; it < NSEG + 1; ++it) {
    lbyte* in = c.L + (it & 1) * 12288;
    if (it < NSEG) {
      *(LAS v4u*)(in + c.tid * 16) = r0;
      if (c.wave < 4) *(LAS v4u*)(in + 8192 + c.tid * 16) = r1;
    }
    VMWAIT(0);
    BAR();
    if (it >= 1 && c.tid == 0) set_line(c, line, (unsigned)it);
    CONV();
    if (it == NSEG) break;
    if (it + 1 < NSEG) {
      if (seen < (unsigned)(it + 2)) {
        wait_lines(c, in_line, (unsigned)(it + 2), 0x21);
        seen = it + 2;
      }
      r0 = ld16(c.rs, in_off + (it + 1) * in_stride + c.tid * 16);
      r1 = ld16(c.rs, in_off + (it + 1) * in_stride + 8192 + (c.tid & 255) * 16);
    }
    const unsigned pv = __hip_atomic_load(c.ctl + in_line * 32, RLX_AGENT);
    f32x4 acc[2][5];
#pragma unroll
    for (int t = 0; t < 5; ++t) { acc[0][t] = (f32x4){0.f, 0.f, 0.f, 0.f}; acc[1][t] = acc[0][t]; }
#pragma unroll
    for (int ks = 0; ks < 6; ++ks) {
      const v4u x0 = lds128(in + ks * 1024 + c.lane * 16), x1 = lds128(in + 6144 + ks * 1024 + c.lane * 16);
#pragma unroll
      for (int t = 0; t < 5; ++t) {
        acc[0][t] = mfma(x0, w[t][ks], acc[0][t]);
        acc[1][t] = mfma(x1, w[t][ks], acc[1][t]);
      }
    }
    seen = max(seen, (unsigned)__builtin_amdgcn_readfirstlane(pv));
    __builtin_amdgcn_fence(__ATOMIC_ACQUIRE, "wavefront");
    const int go = out_off + it * out_stride + c.wave * 5120 + c.lane * 16;
#pragma unroll
    for (int t = 0; t < 5; ++t)
      st16(c.rs, go + t * 1024, (v4u){pk2(acc[0][t][0], acc[0][t][1]), pk2(acc[0][t][2], acc[0][t][3]), pk2(acc[1][t][0], acc[1][t][1]), pk2(acc[1][t][2], acc[1][t][3])});
  }
}

DEV bool ok4(v4u v) { return v.x != SENT && v.y != SENT && v.z != SENT && v.w != SENT; }
DEV bool ok2(v2u v) { return v.x != SENT && v.y != SENT; }
constexpr int C_MEM = 0, C_IMG = 10240, C_RED = 10240 + 12288;

struct GatherA { int off[2]; int dst[2]; };
DEV void gatherA_setup(const Ctx& c, GatherA& g) {
#pragma unroll
  for (int i = 0; i < 2; ++i) {
    int pc = (c.wave * 2 + i) * 64 + c.lane; if (pc >= 640) pc = 0;
    const int r = pc >> 3, rho = r & 3, cs = r >> 2, row = 2 * rho + ((pc >> 2) & 1), c8 = cs * 4 + (pc & 3);
    g.off[i] = R_MEM + pc * 16;
    g.dst[i] = C_MEM + (c8 >> 3) * 1024 + (((c8 >> 1) & 3) * 16 + (c8 & 1) * 8 + row) * 16;
  }
}
DEV void gatherA(const Ctx& c, const Params& p, const GatherA& g, int s, unsigned tgo) {
  if (c.wave < 5) {
    v4u v0, v1;
    if (s == 0) {
      const int pc0 = (c.wave * 2) * 64 + c.lane, pc1 = pc0 + 64;
      v0 = *(const v4u*)(p.mem0 + (2 * ((pc0 >> 3) & 3) + ((pc0 >> 2) & 1)) * 640 + ((pc0 >> 5) * 4 + (pc0 & 3)) * 8);
      v1 = *(const v4u*)(p.mem0 + (2 * ((pc1 >> 3) & 3) + ((pc1 >> 2) & 1)) * 640 + ((pc1 >> 5) * 4 + (pc1 & 3)) * 8);
    } else {
      wait_until(tgo);
      const int base = OFF_RING + (s & 3) * RING_SLOT;
      for (unsigned spins = 0;;) {
        asm volatile("" ::: "memory");
        v0 = ld16(c.rs, base + g.off[0]); v1 = ld16(c.rs, base + g.off[1]);
        if (__all(ok4(v0) && ok4(v1))) break;
        if (spin_fail(spins, c.ctl, 0x100 + s)) break;
      }
    }
    *(LAS v4u*)(c.L + g.dst[0]) = v0;
    *(LAS v4u*)(c.L + g.dst[1]) = v1;
  }
  BAR();
}
DEV void stage1(const Ctx& c, const v8u (&w)[3][5], int idx, f32x4 (&acc)[3]) {
  lbyte* in = c.L + C_MEM + (c.wave >> 2) * 5120 + c.lane * 16;
#pragma unroll
  for (int t = 0; t < 3; ++t) acc[t] = (f32x4){0.f, 0.f, 0.f, 0.f};
#pragma unroll
  for (int ks = 0; ks < 5; ++ks) {
    const v4u a = lds128(in + ks * 1024);
#pragma unroll
    for (int t = 0; t < 3; ++t) acc[t] = smfma(a, w[t][ks], acc[t], idx);
  }
}

struct RState { int osx, osm, ozm, ou; v4u u[4][2]; unsigned seen; };
DEV void r_setup(const Ctx& c, RState& r, int ridx) {
  const int rho = ridx & 3, cs = ridx >> 2, m = c.lane & 15, kq = c.lane >> 4;
  const int h = 4 * c.wave + (m >> 2), row = 2 * rho + ((m >> 1) & 1), g = m & 1;
  r.osx = R_SC + h * 1280 + row * 64 + kq * 16 + g * 8;
  r.osm = R_SC + h * 1280 + 512 + row * 16 + g * 8;
  r.ozm = R_ZM + ((4 * c.wave + kq) * 640 + 32 * cs + m) * 16;
  r.ou = OFF_U + (4 * c.wave * 40 + 2 * cs) * 1024 + c.lane * 16;
  r.seen = 0;
}
DEV void r_prefetch(const Ctx& c, RState& r, int s) {
  if (r.seen < (unsigned)(s + 1)) {
    unsigned v;
    for (unsigned spins = 0;;) {
      v = __hip_atomic_load(c.ctl + (L_PO + 4 * c.wave + (c.lane & 3)) * 32, RLX_AGENT);
      if (__all(v >= (unsigned)(s + 1))) break;
      __builtin_amdgcn_s_sleep(2);
      if (spin_fail(spins, c.ctl, 0x300 + s)) break;
    }
    v = min(min(__builtin_amdgcn_readlane(v, 0), __builtin_amdgcn_readlane(v, 1)), min(__builtin_amdgcn_readlane(v, 2), __builtin_amdgcn_readlane(v, 3)));
    r.seen = v;
    __builtin_amdgcn_fence(__ATOMIC_ACQUIRE, "wavefront");
  }
#pragma unroll
  for (int hh = 0; hh < 4; ++hh)
#pragma unroll
    for (int t = 0; t < 2; ++t) r.u[hh][t] = ld16(c.rs, r.ou + s * (32 * 40960) + hh * 40960 + t * 1024);
}
DEV void r_stage(const Ctx& c, RState& r, int ridx, int s, unsigned tgo) {
  const int m = c.lane & 15, kq = c.lane >> 4, hh0 = m >> 2, g = m & 1;
  const int base = OFF_RING + (s & 3) * RING_SLOT;
  v2u xa, xb, ma, mb; v4u z0, z1;
  wait_until(tgo);
  for (unsigned spins = 0;;) {
    asm volatile("" ::: "memory");
    xa = ld8(c.rs, base + r.osx); xb = ld8(c.rs, base + r.osx + 640);
    ma = ld8(c.rs, base + r.osm); mb = ld8(c.rs, base + r.osm + 640);
    z0 = ld16(c.rs, base + r.ozm); z1 = ld16(c.rs, base + r.ozm + 256);
    if (__all(ok2(xa) && ok2(xb) && ok2(ma) && ok2(mb) && ok4(z0) && ok4(z1))) break;
    if (spin_fail(spins, c.ctl, 0x200 + s)) break;
  }
  float sx[4] = {bflo(xa.x) + bflo(xb.x), bfhi(xa.x) + bfhi(xb.x), bflo(xa.y) + bflo(xb.y), bfhi(xa.y) + bfhi(xb.y)};
  float sm[4] = {bflo(ma.x) + bflo(mb.x), bfhi(ma.x) + bfhi(mb.x), bflo(ma.y) + bflo(mb.y), bfhi(ma.y) + bfhi(mb.y)};
  float mx = fmaxf(fmaxf(fmaxf(sx[0], sx[1]), fmaxf(sx[2], sx[3])), fmaxf(fmaxf(sm[0], sm[1]), fmaxf(sm[2], sm[3])));
  mx = fmaxf(mx, dpp<DPP_XOR1>(mx)); mx = x16_max(mx); mx = x32_max(mx);
#pragma unroll
  for (int j = 0; j < 4; ++j) { sx[j] = __builtin_amdgcn_exp2f(sx[j] - mx); sm[j] = __builtin_amdgcn_exp2f(sm[j] - mx); }
  float l = (sx[0] + sx[1]) + (sx[2] + sx[3]) + 0.25f * ((sm[0] + sm[1]) + (sm[2] + sm[3]));
  l += dpp<DPP_XOR1>(l); l = sw16_add(l, l); l = sw32_add(l, l);
  const float il = __builtin_amdgcn_rcpf(l);
  const unsigned px0 = pk2(sx[0] * il, sx[1] * il), px1 = pk2(sx[2] * il, sx[3] * il);
  const unsigned pm0 = pk2(sm[0] * il, sm[1] * il), pm1 = pk2(sm[2] * il, sm[3] * il);
  f32x4 a0 = {0.f, 0.f, 0.f, 0.f}, a1 = a0;
#pragma unroll
  for (int hh = 0; hh < 4; ++hh) {
    const bool c0 = (hh0 == hh) && !g, c1 = (hh0 == hh) && g;
    const v4u a = {c0 ? px0 : 0u, c0 ? px1 : 0u, c1 ? px0 : 0u, c1 ? px1 : 0u};
    a0 = mfma(a, r.u[hh][0], a0); a1 = mfma(a, r.u[hh][1], a1);
  }
  {
    const bool c0 = (hh0 == kq) && !g, c1 = (hh0 == kq) && g;
    const v4u a = {c0 ? pm0 : 0u, c0 ? pm1 : 0u, c1 ? pm0 : 0u, c1 ? pm1 : 0u};
    a0 = mfma(a, z0, a0); a1 = mfma(a, z1, a1);
  }
  const float v00 = a0[0] + a0[1], v01 = a0[2] + a0[3], v10 = a1[0] + a1[1], v11 = a1[2] + a1[3];
  const float w0 = sw32_add(v00, v01), w1 = sw32_add(v10, v11);
  const float o = sw16_add(w0, w1);
  *(LAS float*)(c.L + C_RED + c.wave * 256 + c.lane * 4) = o;
  BAR();
  if (c.wave == 0) {
    const f32x4 p0 = *(LAS f32x4*)(c.L + C_RED + kq * 512 + m * 16), p1 = *(LAS f32x4*)(c.L + C_RED + kq * 512 + 256 + m * 16);
    f32x4 t = p0 + p1;
    const float y0 = sw32_add(t[0], t[2]), y1 = sw32_add(t[1], t[3]);
    const float e0 = sw16_add(y0, y0), e1 = sw16_add(y1, y1);
    const unsigned d = nosent(pk2(e0, e1));
    auto sr = __builtin_amdgcn_permlane32_swap(d, d, false, false);
    const v2u dd = {(unsigned)sr[0], (unsigned)sr[1]};
    if (c.lane < 16) {
      st8(c.rs, OFF_RING + ((s + 1) & 3) * RING_SLOT + R_MEM + ridx * 128 + m * 8, dd);
      st8(c.rs, OFF_RING + ((s + 3) & 3) * RING_SLOT + R_MEM + ridx * 128 + m * 8, (v2u){SENT, SENT});
    }
  }
  CONV();
}
DEV void arm_initial(const Ctx& c, int off, int npieces) {
#pragma unroll
  for (int sl = 0; sl < 4; ++sl)
    if (c.tid < npieces) st16(c.rs, OFF_RING + sl * RING_SLOT + off + c.tid * 16, (v4u){SENT, SENT, SENT, SENT});
}

DEV void role_S(const Ctx& c, const Params& p, int h, int g, int ridx) {
  v8u w[3][5];
  load_wA<true>(c, w, p.wq + h * 192 + g * 96, p.wk + h * 192 + g * 96, 6144, 12);
  start_wait(c);
  GatherA ga; gatherA_setup(c, ga);
  RState r; r_setup(c, r, ridx);
  const int kh = c.wave >> 2, fg = c.wave & 3, n = c.lane & 15, rq = c.lane >> 4, qq = (c.lane >> 2) & 3, pp = c.lane & 3;
  const int idx = (n < 8) ? 0x4444 : 0xEEEE;
  const int sid = h * 2 + g;
  lbyte* iw01 = c.L + C_IMG + (48 * fg + 16 * (c.lane >> 5) + n) * 32 + kh * 16 + (rq & 1) * 8;
  lbyte* iw2 = c.L + C_IMG + (48 * fg + 32 + n) * 32 + kh * 16 + (rq & 1) * 8 + (c.lane >> 5) * 4;
  lbyte* ir = c.L + C_IMG + (8 * rq + qq) * 32 + pp * 8;
  v4u kx[2][3];
  unsigned kseen = 0;
  unsigned tref = 0;
  for (int s = 0; s < NSEG; ++s) {
    const bool last = s == NSEG - 1;
    if (!last) {
      if (c.wave == 0) {
        if (kseen < (unsigned)(s + 1)) { wait_lines(c, L_PK + h, (unsigned)(s + 1), 0x400 + s); kseen = __builtin_amdgcn_readfirstlane(__hip_atomic_load(c.ctl + (L_PK + h) * 32, RLX_AGENT)); __builtin_amdgcn_fence(__ATOMIC_ACQUIRE, "wavefront"); }
#pragma unroll
        for (int kt = 0; kt < 2; ++kt)
#pragma unroll
          for (int ks = 0; ks < 3; ++ks) kx[kt][ks] = ld16(c.rs, OFF_K + (s * 32 + h) * 12288 + (kt * 6 + 3 * g + ks) * 1024 + c.lane * 16);
      }
      r_prefetch(c, r, s);
    }
    gatherA(c, p, ga, s, tref + p.dA);
    if (s >= 1 && c.tid == 64) set_line(c, L_PS + sid, (unsigned)s);
    f32x4 acc[3];
    stage1(c, w, idx, acc);
    {
      const float a0 = sw32_add(acc[0][0], acc[1][0]), a1 = sw32_add(acc[0][1], acc[1][1]), a2 = sw32_add(acc[0][2], acc[1][2]), a3 = sw32_add(acc[0][3], acc[1][3]);
      const float b0 = sw32_add(acc[2][0], acc[2][2]), b1 = sw32_add(acc[2][1], acc[2][3]);
      *(LAS v2u*)iw01 = (v2u){pk2(a0, a1), pk2(a2, a3)};
      *(LAS unsigned*)iw2 = pk2(b0, b1);
    }
    BAR();
    const int base = OFF_RING + (s & 3) * RING_SLOT + R_SC + sid * 640;
    const int base2 = OFF_RING + ((s + 2) & 3) * RING_SLOT + R_SC + sid * 640;
    if (c.wave < 2) {
      v4u qf[3];
#pragma unroll
      for (int ks = 0; ks < 3; ++ks) { const v2u r0 = trd(ir + ks * 1024), r1 = trd(ir + ks * 1024 + 128); qf[ks] = (v4u){r0.x, r0.y, r1.x, r1.y}; }
      if (c.wave == 0) {
        if (!last) {
          f32x4 c0 = {0.f, 0.f, 0.f, 0.f}, c1 = c0;
#pragma unroll
          for (int ks = 0; ks < 3; ++ks) { c0 = mfma(kx[0][ks], qf[ks], c0); c1 = mfma(kx[1][ks], qf[ks], c1); }
#pragma unroll
          for (int j = 0; j < 4; ++j) { c0[j] = (c0[j] + dpp<DPP_ROR8>(c0[j])) * SCL2; c1[j] = (c1[j] + dpp<DPP_ROR8>(c1[j])) * SCL2; }
          if (n < 8) {
            st16(c.rs, base + n * 64 + rq * 16, (v4u){nosent(pk2(c0[0], c0[1])), nosent(pk2(c0[2], c0[3])), nosent(pk2(c1[0], c1[1])), nosent(pk2(c1[2], c1[3]))});
            st16(c.rs, base2 + n * 64 + rq * 16, (v4u){SENT, SENT, SENT, SENT});
          }
        }
      } else {
        v4u kf[3];
#pragma unroll
        for (int ks = 0; ks < 3; ++ks) { const v2u r0 = trd(ir + 3072 + ks * 1024), r1 = trd(ir + 3072 + ks * 1024 + 128); kf[ks] = (v4u){r0.x, r0.y, r1.x, r1.y}; }
        if (!last) {
          f32x4 c0 = {0.f, 0.f, 0.f, 0.f};
#pragma unroll
          for (int ks = 0; ks < 3; ++ks) c0 = mfma(kf[ks], qf[ks], c0);
#pragma unroll
          for (int j = 0; j < 4; ++j) { c0[j] += dpp<DPP_ROR8>(c0[j]); c0[j] = sw32_add(c0[j], c0[j]) * SCL2; }
          if (n < 8 && rq < 2) {
            st8(c.rs, base + 512 + n * 16 + rq * 8, (v2u){nosent(pk2(c0[0], c0[1])), nosent(pk2(c0[2], c0[3]))});
            st8(c.rs, base2 + 512 + n * 16 + rq * 8, (v2u){SENT, SENT});
          }
        }
#pragma unroll
        for (int ks = 0; ks < 3; ++ks) st16(c.rs, OFF_KM + (s * 32 + h) * 6144 + (3 * g + ks) * 1024 + c.lane * 16, kf[ks]);
      }
    }
    if (last) break;
    tref = ct32();
    r_stage(c, r, ridx, s, tref + p.dB);
    tref = ct32();
  }
  if (c.wave == 1) { VMWAIT(0); if (c.lane == 0) set_line(c, L_PS + sid, (unsigned)NSEG); }
}

DEV void role_UV(const Ctx& c, const Params& p, int h, int g) {
  v8u w[3][5], wz[3][3];
  const int tile0 = c.wave < 4 ? 3 * c.wave : 12 + 2 * (c.wave - 4);
  load_wA<true>(c, w, p.wv + h * 192, p.wv, 6144, 24);
  load_wZ(c, wz, p.wo + (size_t)h * 192 * 640 + g * 320, tile0);
  for (int i = c.tid; i < 6144 / 4; i += 512) *(LAS unsigned*)(c.L + C_IMG + i * 4) = 0u;
  start_wait(c);
  GatherA ga; gatherA_setup(c, ga);
  const int fg = c.wave & 3, n = c.lane & 15, rq = c.lane >> 4, qq = (c.lane >> 2) & 3, pp = c.lane & 3;
  const int idx = (n < 8) ? 0x4444 : 0xEEEE;
  const int uid = h * 2 + g;
  const int iw01 = C_IMG + (48 * fg + 16 * (c.lane >> 5) + n) * 16 + (rq & 1) * 8;
  const int iw2 = C_IMG + (48 * fg + 32 + n) * 16 + (rq & 1) * 8 + (c.lane >> 5) * 4;
  const int ir = C_IMG + (16 * rq + 8 * (pp >> 1) + qq) * 16 + (pp & 1) * 8;
  const int zo = (h * 640 + g * 320 + tile0 * 16) * 16;
  unsigned tref = 0;
  for (int s = 0; s < NSEG; ++s) {
    VMWAIT(0);
    gatherA(c, p, ga, s, tref + p.dN);
    if (s >= 1 && c.tid == 0) set_line(c, L_PUV + uid, (unsigned)s);
    f32x4 acc[3];
    stage1(c, w, idx, acc);
    lbyte* im = c.L + (s & 1) * 3072;
    {
      const float a0 = sw32_add(acc[0][0], acc[1][0]), a1 = sw32_add(acc[0][1], acc[1][1]), a2 = sw32_add(acc[0][2], acc[1][2]), a3 = sw32_add(acc[0][3], acc[1][3]);
      const float b0 = sw32_add(acc[2][0], acc[2][2]), b1 = sw32_add(acc[2][1], acc[2][3]);
      __builtin_amdgcn_ds_atomic_fadd_v2bf16((LAS s16x2*)(im + iw01), __builtin_bit_cast(s16x2, pk2(a0, a1)));
      __builtin_amdgcn_ds_atomic_fadd_v2bf16((LAS s16x2*)(im + iw01 + 4), __builtin_bit_cast(s16x2, pk2(a2, a3)));
      __builtin_amdgcn_ds_atomic_fadd_v2bf16((LAS s16x2*)(im + iw2), __builtin_bit_cast(s16x2, pk2(b0, b1)));
    }
    BAR();
    f32x4 z[3];
#pragma unroll
    for (int t = 0; t < 3; ++t) z[t] = (f32x4){0.f, 0.f, 0.f, 0.f};
#pragma unroll
    for (int ks = 0; ks < 3; ++ks) {
      const v2u r0 = trd(im + ir + ks * 1024), r1 = trd(im + ir + ks * 1024 + 64);
      const v4u a = {r0.x, r0.y, r1.x, r1.y};
      z[0] = smfma(a, wz[0][ks], z[0], idx);
      z[1] = smfma(a, wz[1][ks], z[1], idx);
      if (c.wave < 4) z[2] = smfma(a, wz[2][ks], z[2], idx);
    }
    const int base = OFF_RING + (s & 3) * RING_SLOT + R_ZM + zo, base2 = OFF_RING + ((s + 2) & 3) * RING_SLOT + R_ZM + zo;
    const int o01 = ((c.lane >> 5) * 16 + n) * 16 + (rq & 1) * 8, o2 = (32 + n) * 16 + (rq & 1) * 8;
    const v2u d01 = {nosent(pk2(sw32_add(z[0][0], z[1][0]), sw32_add(z[0][1], z[1][1]))), nosent(pk2(sw32_add(z[0][2], z[1][2]), sw32_add(z[0][3], z[1][3])))};
    st8(c.rs, base + o01, d01);
    v2u d2 = {0u, 0u};
    if (c.wave < 4) {
      d2 = (v2u){nosent(pk2(sw32_add(z[2][0], z[2][0]), sw32_add(z[2][1], z[2][1]))), nosent(pk2(sw32_add(z[2][2], z[2][2]), sw32_add(z[2][3], z[2][3])))};
      if (c.lane < 32) st8(c.rs, base + o2, d2);
    }
    tref = ct32();
    st8(c.rs, base2 + o01, (v2u){SENT, SENT});
    st8(c.rs, OFF_ZMP + s * 327680 + zo + o01, d01);
    if (c.wave < 4 && c.lane < 32) { st8(c.rs, base2 + o2, (v2u){SENT, SENT}); st8(c.rs, OFF_ZMP + s * 327680 + zo + o2, d2); }
    if (c.tid < 192) *(LAS v4u*)(c.L + C_IMG + ((s + 1) & 1) * 3072 + c.tid * 16) = (v4u){0u, 0u, 0u, 0u};
    CONV();
  }
  VMWAIT(0);
  BAR();
  if (c.tid == 0) set_line(c, L_PUV + uid, (unsigned)NSEG);
}

DEV void role_R(const Ctx& c, const Params& p, int ridx) {
  start_wait(c);
  RState r; r_setup(c, r, ridx);
  unsigned tref = ct32();
  for (int s = 0; s < NSEG - 1; ++s) {
    r_prefetch(c, r, s);
    r_stage(c, r, ridx, s, tref + p.dN);
    tref = ct32();
    BAR();
  }
}

DEV void task_PS(const Ctx& c, int s, int hg) {
  const int h = 4 * hg + (c.wave >> 1), rt = c.wave & 1, n = c.lane & 15, rq = c.lane >> 4;
  const int part = s < 22 ? 0 : (s < 43 ? 1 : 2);
  {
    const int l3 = c.lane & 3;
    const int line = l3 == 0 ? L_PK + h : (l3 == 1 ? L_PQ + h * 3 + part : L_PS + h * 2 + (l3 - 2));
    wait_lines(c, line, (unsigned)(s + 1), 0x500 + s);
  }
  v4u qf[6], k0[6], k1[6], km[6];
  const int bq = OFF_Q + (s * 32 + h) * 12288 + rt * 6144 + c.lane * 16, bk = OFF_K + (s * 32 + h) * 12288 + c.lane * 16, bm = OFF_KM + (s * 32 + h) * 6144 + c.lane * 16;
#pragma unroll
  for (int ks = 0; ks < 6; ++ks) { qf[ks] = ld16(c.rs, bq + ks * 1024); k0[ks] = ld16(c.rs, bk + ks * 1024); km[ks] = ld16(c.rs, bm + ks * 1024); k1[ks] = ld16(c.rs, bk + (rt ? 6144 : 0) + ks * 1024); }
  f32x4 c0 = {0.f, 0.f, 0.f, 0.f}, c1 = c0, cm = c0;
#pragma unroll
  for (int ks = 0; ks < 6; ++ks) { c0 = mfma(k0[ks], qf[ks], c0); c1 = mfma(k1[ks], qf[ks], c1); cm = mfma(km[ks], qf[ks], cm); }
  const int row = 16 * rt + n;
  float mx = -3.0e38f;
#pragma unroll
  for (int j = 0; j < 4; ++j) {
    cm[j] = sw32_add(cm[j], cm[j]) * SCL2;
    c0[j] = (4 * rq + j <= row) ? c0[j] * SCL2 : -INFINITY;
    c1[j] = (16 + 4 * rq + j <= row) ? c1[j] * SCL2 : -INFINITY;
    mx = fmaxf(mx, fmaxf(cm[j], fmaxf(c0[j], c1[j])));
  }
  mx = x16_max(mx); mx = x32_max(mx);
  float l = 0.f;
#pragma unroll
  for (int j = 0; j < 4; ++j) {
    c0[j] = __builtin_amdgcn_exp2f(c0[j] - mx); c1[j] = __builtin_amdgcn_exp2f(c1[j] - mx); cm[j] = __builtin_amdgcn_exp2f(cm[j] - mx);
    l += c0[j] + c1[j] + 0.5f * cm[j];
  }
  l = sw16_add(l, l); l = sw32_add(l, l);
  const float il = __builtin_amdgcn_rcpf(l);
  st16(c.rs, OFF_P + ((s * 32 + h) * 2 + rt) * 1024 + c.lane * 16,
       (v4u){pk2(c0[0] * il, c0[1] * il), pk2(c0[2] * il, c0[3] * il), pk2(c1[0] * il, c1[1] * il), pk2(c1[2] * il, c1[3] * il)});
  if (rq < 2) st8(c.rs, OFF_PM + ((s * 8 + hg) * 2 + rt) * 1024 + ((c.wave >> 1) * 16 + n) * 16 + rq * 8, (v2u){pk2(cm[0] * il, cm[1] * il), pk2(cm[2] * il, cm[3] * il)});
  VMWAIT(0);
  BAR();
  if (c.tid == 0) __hip_atomic_fetch_add(c.ctl + (L_PSC + s) * 32, 1u, RLX_AGENT);
  CONV();
}
DEV void task_POST(const Ctx& c, const Params& p, int s, int cb) {
  const int n = c.lane & 15, rq = c.lane >> 4;
  {
    const int l = c.lane & 15;
    const int line = l < 4 ? L_PO + 4 * c.wave + l : (l < 8 ? L_PUV + (4 * c.wave + l - 4) * 2 + (cb >> 2) : L_PG + c.wave);
    wait_lines(c, line, (unsigned)(s + 1), 0x600 + s);
    wait_lines(c, L_PSC + s, 8u, 0x700 + s);
  }
  f32x4 acc[2][5];
#pragma unroll
  for (int t = 0; t < 5; ++t) { acc[0][t] = (f32x4){0.f, 0.f, 0.f, 0.f}; acc[1][t] = acc[0][t]; }
  {
    v4u pf[5][2], uf[5][5];
#pragma unroll
    for (int hh = 0; hh < 4; ++hh) {
      const int h = 4 * c.wave + hh;
#pragma unroll
      for (int rt = 0; rt < 2; ++rt) pf[hh][rt] = ld16(c.rs, OFF_P + ((s * 32 + h) * 2 + rt) * 1024 + c.lane * 16);
#pragma unroll
      for (int t = 0; t < 5; ++t) uf[hh][t] = ld16(c.rs, OFF_U + (s * 32 + h) * 40960 + (5 * cb + t) * 1024 + c.lane * 16);
    }
#pragma unroll
    for (int rt = 0; rt < 2; ++rt) pf[4][rt] = ld16(c.rs, OFF_PM + ((s * 8 + c.wave) * 2 + rt) * 1024 + c.lane * 16);
#pragma unroll
    for (int t = 0; t < 5; ++t) uf[4][t] = ld16(c.rs, OFF_ZMP + s * 327680 + ((4 * c.wave + rq) * 640 + 80 * cb + 16 * t + n) * 16);
#pragma unroll
    for (int k = 0; k < 5; ++k)
#pragma unroll
      for (int t = 0; t < 5; ++t) { acc[0][t] = mfma(uf[k][t], pf[k][0], acc[0][t]); acc[1][t] = mfma(uf[k][t], pf[k][1], acc[1][t]); }
  }
  v4u gf[5];
#pragma unroll
  for (int t = 0; t < 5; ++t) gf[t] = ld16(c.rs, OFF_G + (s * 8 + c.wave) * 40960 + (5 * cb + t) * 1024 + c.lane * 16);
#pragma unroll
  for (int rt = 0; rt < 2; ++rt)
#pragma unroll
    for (int t = 0; t < 5; ++t) *(LAS f32x4*)(c.L + ((rt * 5 + t) * 8 + c.wave) * 1024 + c.lane * 16) = acc[rt][t];
  BAR();
  f32x4 sv[2];
#pragma unroll
  for (int k = 0; k < 2; ++k) {
    sv[k] = (f32x4){0.f, 0.f, 0.f, 0.f};
    if (k == 0 || c.wave < 2) {
#pragma unroll
      for (int j = 0; j < 8; ++j) sv[k] += *(LAS f32x4*)(c.L + ((k * 8 + c.wave) * 8 + j) * 1024 + c.lane * 16);
    }
  }
  BAR();
  {
    const unsigned one = (rq == (n >> 2)) ? ((n & 1) ? 0x3F800000u : 0x00003F80u) : 0u;
    const unsigned ia = (n & 2) ? 0u : one, ib = (n & 2) ? one : 0u;
    const v4u id0 = {ia, ib, 0u, 0u}, id1 = {0u, 0u, ia, ib};
    const f32x4 zero = {0.f, 0.f, 0.f, 0.f};
#pragma unroll
    for (int t = 0; t < 5; ++t) {
      *(LAS f32x4*)(c.L + (t * 8 + c.wave) * 1024 + c.lane * 16) = mfma(gf[t], id0, zero);
      *(LAS f32x4*)(c.L + ((5 + t) * 8 + c.wave) * 1024 + c.lane * 16) = mfma(gf[t], id1, zero);
    }
  }
  BAR();
#pragma unroll
  for (int k = 0; k < 2; ++k) {
    if (k == 0 || c.wave < 2) {
      const int u = k * 8 + c.wave, rt = u / 5, t = u % 5;
      f32x4 gv = {0.f, 0.f, 0.f, 0.f};
#pragma unroll
      for (int j = 0; j < 8; ++j) gv += *(LAS f32x4*)(c.L + (u * 8 + j) * 1024 + c.lane * 16);
      const int col = 80 * cb + 16 * t + 4 * rq, row = 32 * s + 16 * rt + n;
      const v2u bb = *(const v2u*)(p.b2 + col), xx = *(const v2u*)(p.x + (size_t)row * 640 + col);
      const float b[4] = {bflo(bb.x), bfhi(bb.x), bflo(bb.y), bfhi(bb.y)}, xv[4] = {bflo(xx.x), bfhi(xx.x), bflo(xx.y), bfhi(xx.y)};
      float o[4];
#pragma unroll
      for (int j = 0; j < 4; ++j) {
        const float u2 = rbf(rbf(gv[j]) + b[j]);
        const float gate = rbf(u2 * __builtin_amdgcn_rcpf(1.0f + __builtin_amdgcn_exp2f(-1.4426950408889634f * u2)));
        o[j] = xv[j] + rbf(rbf(sv[k][j]) * gate);
      }
      *(v2u*)(p.out + (size_t)row * 640 + col) = (v2u){pk2(o[0], o[1]), pk2(o[2], o[3])};
    }
  }
  BAR();
}
DEV void task_loop(const Ctx& c, const Params& p) {
  for (;;) {
    CONV();
    if (c.tid == 0) *(LAS unsigned*)(c.L + LDS_BYTES - 16) = __hip_atomic_fetch_add(c.ctl + L_QUEUE * 32, 1u, RLX_AGENT);
    BAR();
    const unsigned t = __builtin_amdgcn_readfirstlane(*(LAS unsigned*)(c.L + LDS_BYTES - 16));
    BAR();
    if (t >= NSEG * 16) break;
    const int s = t >> 4, k = t & 15;
    if (k < 8) task_PS(c, s, k); else task_POST(c, p, s, k - 8);
  }
}

enum { RB_LN = 1, RB_KV = 2, RB_O = 4, RB_CHAIN = 8, RB_Q = 16, RB_TASK = 32 };

__global__ void __launch_bounds__(512, 2) rk_kernel(Params p) {
  extern __shared__ __attribute__((aligned(16))) unsigned char lds[];
  Ctx c;
  c.rs = __builtin_amdgcn_make_buffer_rsrc(p.ws, 0, WS_END, 0x00020000);
  c.ctl = (gu32*)p.ws;
  c.L = (lbyte*)lds;
  c.l0 = (unsigned)(uintptr_t)c.L;
  c.tid = threadIdx.x; c.wave = __builtin_amdgcn_readfirstlane(threadIdx.x >> 6); c.lane = threadIdx.x & 63;
  const int b = blockIdx.x, x = b & 7, j = b >> 3;
  const int ridx = x * 10 + (j < 8 ? j : j - 20);
  if (p.roles & RB_LN) {
    phase_ln(c, p);
    if (j < 8) { arm_initial(c, R_SC + ((x + 8 * (j >> 1)) * 2 + (j & 1)) * 640, 40); }
    else if (j < 16) arm_initial(c, R_ZM + ((x + 8 * ((j - 8) >> 1)) * 640 + (j & 1) * 320) * 16, 320);
    if (j < 8 || j == 28 || j == 29) arm_initial(c, R_MEM + ridx * 128, 8);
    start_arrive(c);
  }
  if (j < 8) {
    if (p.roles & RB_CHAIN) role_S(c, p, x + 8 * (j >> 1), j & 1, ridx);
  } else if (j < 16) {
    if (p.roles & RB_CHAIN) role_UV(c, p, x + 8 * ((j - 8) >> 1), j & 1);
  } else if (j == 28 || j == 29) {
    if (p.roles & RB_CHAIN) role_R(c, p, ridx);
  } else {
    const int h = x + 8 * (j & 3);
    bool started = false;
    if ((j >= 24 && j < 28) || j == 31) {
      if (p.roles & RB_O) {
        v4u w[5][6];
        load_wB(c, w, j == 31 ? p.w2 + (size_t)x * 192 * 640 : p.wo + (size_t)h * 192 * 640);
        if (j == 31) stream_B(c, w, OFF_H + x * 12288, 8 * 12288, L_PH + x, OFF_G + x * 40960, 8 * 40960, L_PG + x);
        else stream_B(c, w, OFF_V + h * 12288, 32 * 12288, L_PV + h, OFF_U + h * 40960, 32 * 40960, L_PO + h);
      }
    } else if (j == 30) {
      if (p.roles & RB_KV) {
        v8u w[3][5];
        load_wA<false>(c, w, p.w1 + x * 192, p.w1, 1536, 24);
        start_wait(c); started = true;
        stream_A<true>(c, p, w, OFF_Y, 0, NSEG, OFF_H + x * 12288, 8 * 12288, L_PH + x, p.b1 + x * 192);
      }
    }
    if (j < 28) {
      for (int pass = 0; pass < 2; ++pass) {
        if (pass == 0 ? (j >= 24 || !(p.roles & RB_KV)) : !(p.roles & RB_Q)) continue;
        const int part = (j - 16) >> 2;
        const u16* wsrc = pass ? p.wq : (j < 20 ? p.wk : p.wv);
        v8u w[3][5];
        load_wA<false>(c, w, wsrc + h * 192, wsrc, 6144, 24);
        if (!started) { start_wait(c); started = true; }
        const int s0 = pass ? (part == 0 ? 0 : (part == 1 ? 22 : 43)) : 0, s1 = pass ? (part == 0 ? 22 : (part == 1 ? 43 : 64)) : NSEG;
        const int ooff = pass ? OFF_Q : (j < 20 ? OFF_K : OFF_V);
        const int line = pass ? L_PQ + h * 3 + part : (j < 20 ? L_PK + h : L_PV + h);
        stream_A<false>(c, p, w, OFF_XN, s0, s1, ooff + h * 12288, 32 * 12288, line, nullptr);
      }
    }
    if (p.roles & RB_TASK) task_loop(c, p);
  }
}
}

extern "C" void kernel_launch(void* const* d_in, const int* in_sizes, int n_in, void* d_out, int out_size, void* d_ws, size_t ws_size,
                              hipStream_t stream) {
  using namespace rk;
  (void)in_sizes; (void)n_in; (void)out_size; (void)ws_size;
  hipFuncSetAttribute((const void*)rk_kernel, hipFuncAttributeMaxDynamicSharedMemorySize, LDS_BYTES);
  Params p;
  p.x = (const u16*)d_in[0]; p.mem0 = (const u16*)d_in[1]; p.wq = (const u16*)d_in[2]; p.wk = (const u16*)d_in[3];
  p.wv = (const u16*)d_in[4]; p.wo = (const u16*)d_in[5]; p.w1 = (const u16*)d_in[8]; p.b1 = (const u16*)d_in[9];
  p.w2 = (const u16*)d_in[10]; p.b2 = (const u16*)d_in[11];
  p.out = (u16*)d_out; p.ws = (unsigned char*)d_ws;
  p.dA = 700; p.dB = 700; p.dN = 3600;
  hipMemsetAsync(d_ws, 0, CTRL_BYTES, stream);
  p.roles = 63;
  rk_kernel<<<256, 512, LDS_BYTES, stream>>>(p);
}
```

```cpp
#include <hip/hip_runtime.h>
#include <hip/hip_bf16.h>
#include <stdint.h>

#define GAS __attribute__((address_space(1)))
#define LAS __attribute__((address_space(3)))
typedef unsigned short u16;
typedef unsigned v2u __attribute__((ext_vector_type(2)));
typedef unsigned v4u __attribute__((ext_vector_type(4)));
typedef unsigned v8u __attribute__((ext_vector_type(8)));
typedef float f32x4 __attribute__((ext_vector_type(4)));
typedef __bf16 bf16x2 __attribute__((ext_vector_type(2)));
typedef __bf16 bf16x8 __attribute__((ext_vector_type(8)));
typedef __bf16 bf16x16 __attribute__((ext_vector_type(16)));
typedef short s16x4 __attribute__((ext_vector_type(4)));
typedef GAS unsigned gu32;
typedef GAS unsigned long long gu64;
typedef LAS unsigned char lbyte;
#define RLX_AGENT __ATOMIC_RELAXED, __HIP_MEMORY_SCOPE_AGENT
#define DEV __device__ __forceinline__

namespace rk {
constexpr int NSEG = 64, NW = 60;
constexpr int CTRL_BYTES = 65536;
constexpr int ZM_SLOT = 327680;
constexpr int OFF_XN = CTRL_BYTES;
constexpr int OFF_Y = OFF_XN + 2621440;
constexpr int OFF_V = OFF_Y + 2621440;
constexpr int OFF_H = OFF_V + 25165824;
constexpr int OFF_U = OFF_H + 6291456;
constexpr int OFF_G = OFF_U + 83886080;
constexpr int OFF_ZMP = OFF_G + 20971520;
constexpr int OFF_SX = OFF_ZMP + 20971520;
constexpr int OFF_P = OFF_SX + 10485760;
constexpr int OFF_PM = OFF_P + 4194304;
constexpr int OFF_NR = OFF_PM + 1048576;
constexpr int WS_END = OFF_NR + 4194304;
constexpr int NR_SLOT = 131072, NR_CAL = 4 * NR_SLOT, NR_STAMP = 16384 + 4096, NR_ZM = NR_CAL + 65536;
static_assert(NR_ZM + 4 * ZM_SLOT <= 2097152, "near region");
static_assert(WS_END <= 256 * 1024 * 1024, "workspace");
constexpr int L_TMO = 0, L_START = 1, L_VOTE = 10, L_YCNT = 11, L_ZERO = 12, L_PV = 16, L_SD = 48, L_PO = 64, L_PS = 128, L_PUV = 192, L_PP = 256, L_PH = 320, L_GT = 352;
constexpr int L_EPOCH = 400, L_READY = 401;
constexpr unsigned SENT = 0xFFFFFFFFu;
constexpr unsigned SPIN_CAP = 1u << 17;
constexpr int LDS_BYTES = 155648;
constexpr float SCL2 = 0.07216878364870322f * 1.4426950408889634f;

constexpr int KSPLIT = 12;
constexpr int P_T0 = 38, P_A0 = 24;
constexpr int P_VIS = 4, P_TM = 2, P_AM = 7;
constexpr int P_LATE = 26, P_LATE_CAP = 20;
constexpr int P_STAMP = 20, P_DM = 1, P_DZ = 8, P_CAP_R = 3, P_CAP_A = 2, P_NAP = 2;

struct Params {
  const u16 *x, *mem0, *wq, *wk, *wv, *wo, *w1, *b1, *w2, *b2;
  u16* out;
  unsigned char* ws;
};

DEV float bflo(unsigned u) { return __builtin_bit_cast(float, u << 16); }
DEV float bfhi(unsigned u) { return __builtin_bit_cast(float, u & 0xFFFF0000u); }
DEV unsigned pk2(float a, float b) { bf16x2 v; v[0] = (__bf16)a; v[1] = (__bf16)b; return __builtin_bit_cast(unsigned, v); }
DEV float rbf(float a) { return (float)(__bf16)a; }
DEV unsigned nosent(unsigned u) { return u < 0xFFFFFFFEu ? u : 0xFFFFFFFEu; }
DEV f32x4 mfma(v4u a, v4u b, f32x4 c) {
  return __builtin_amdgcn_mfma_f32_16x16x32_bf16(__builtin_bit_cast(bf16x8, a), __builtin_bit_cast(bf16x8, b), c, 0, 0, 0);
}
DEV f32x4 smfma(v4u a, v8u b, f32x4 c, int idx) {
  return __builtin_amdgcn_smfmac_f32_16x16x64_bf16(__builtin_bit_cast(bf16x8, a), __builtin_bit_cast(bf16x16, b), c, idx, 0, 0);
}
DEV v4u lo4(v8u w) { return __builtin_shufflevector(w, w, 0, 1, 2, 3); }
DEV v4u hi4(v8u w) { return __builtin_shufflevector(w, w, 4, 5, 6, 7); }
DEV v2u trd(lbyte* p) { return __builtin_bit_cast(v2u, __builtin_amdgcn_ds_read_tr16_b64_v4i16((LAS s16x4*)p)); }
DEV v4u lds128(lbyte* p) { return *(LAS v4u*)p; }
DEV void glds16(const void* gsrc, unsigned lds_dst) {
  unsigned keep;
  asm volatile("s_mov_b32 %0, m0\n\ts_mov_b32 m0, %2\n\ts_nop 0\n\tglobal_load_lds_dwordx4 %1, off\n\ts_mov_b32 m0, %0"
               : "=&s"(keep) : "v"(gsrc), "s"(lds_dst) : "memory");
}
#define VMWAIT(n) asm volatile("s_waitcnt vmcnt(" #n ")" ::: "memory")
#define CONV() __builtin_amdgcn_wave_barrier()
#define BAR() do { asm volatile("s_waitcnt lgkmcnt(0)" ::: "memory"); __builtin_amdgcn_s_barrier(); asm volatile("" ::: "memory"); } while (0)
typedef __amdgpu_buffer_rsrc_t rsrc_t;
DEV v4u ld16(rsrc_t rs, int off) { return __builtin_amdgcn_raw_buffer_load_b128(rs, off, 0, 16); }
DEV v2u ld8(rsrc_t rs, int off) { return __builtin_amdgcn_raw_buffer_load_b64(rs, off, 0, 16); }
DEV void st16(rsrc_t rs, int off, v4u v) { __builtin_amdgcn_raw_buffer_store_b128(v, rs, off, 0, 16); }
DEV void st8(rsrc_t rs, int off, v2u v) { __builtin_amdgcn_raw_buffer_store_b64(v, rs, off, 0, 16); }
DEV unsigned rt32() { return (unsigned)__builtin_amdgcn_s_memrealtime(); }
DEV float sw32_add(float a, float b) {
  auto r = __builtin_amdgcn_permlane32_swap(__builtin_bit_cast(unsigned, a), __builtin_bit_cast(unsigned, b), false, false);
  return __builtin_bit_cast(float, (unsigned)r[0]) + __builtin_bit_cast(float, (unsigned)r[1]);
}
DEV float sw16_add(float a, float b) {
  auto r = __builtin_amdgcn_permlane16_swap(__builtin_bit_cast(unsigned, a), __builtin_bit_cast(unsigned, b), false, false);
  return __builtin_bit_cast(float, (unsigned)r[0]) + __builtin_bit_cast(float, (unsigned)r[1]);
}
DEV float x32_max(float a) {
  auto r = __builtin_amdgcn_permlane32_swap(__builtin_bit_cast(unsigned, a), __builtin_bit_cast(unsigned, a), false, false);
  return fmaxf(__builtin_bit_cast(float, (unsigned)r[0]), __builtin_bit_cast(float, (unsigned)r[1]));
}
DEV float x16_max(float a) {
  auto r = __builtin_amdgcn_permlane16_swap(__builtin_bit_cast(unsigned, a), __builtin_bit_cast(unsigned, a), false, false);
  return fmaxf(__builtin_bit_cast(float, (unsigned)r[0]), __builtin_bit_cast(float, (unsigned)r[1]));
}
template <int CTRL> DEV float dpp(float v) {
  return __builtin_bit_cast(float, __builtin_amdgcn_update_dpp(0, __builtin_bit_cast(int, v), CTRL, 0xf, 0xf, true));
}
constexpr int DPP_XOR1 = 0xB1, DPP_XOR2 = 0x4E, DPP_ROR8 = 0x128, DPP_ROR4 = 0x124;
DEV float wave_sum(float v) {
  v += dpp<DPP_XOR1>(v); v += dpp<DPP_XOR2>(v); v += dpp<DPP_ROR4>(v); v += dpp<DPP_ROR8>(v);
  v = sw16_add(v, v); v = sw32_add(v, v);
  return v;
}

struct Ctx {
  rsrc_t rs;
  gu32* ctl;
  lbyte* L;
  unsigned l0;
  int tid, wave, lane;
  int nr;
};
DEV int nr_mem(int r) { return (r >> 6) * 16384 + (r & 63) * 128; }
DEV int nr_zm(int h, int col, int hi) { return NR_ZM + (((col >> 5) % 10) * 2 + (h >> 4)) * 16384 + hi + (h & 15) * 512 + (col & 31) * 16; }
DEV int nr_sc(int h) { return (2 + h / 6) * 16384 + (h % 6) * 1280; }
DEV bool spin_fail(unsigned& spins, gu32* ctl, unsigned code) {
  if (++spins > SPIN_CAP) { __hip_atomic_store(ctl, 1u, RLX_AGENT); __hip_atomic_fetch_max(ctl + 1, code, RLX_AGENT); return true; }
  return (spins & 63u) == 0u && __builtin_amdgcn_readfirstlane(__hip_atomic_load(ctl, RLX_AGENT)) != 0;
}
DEV void wait_lines(const Ctx& c, int line, unsigned want, unsigned code, int nap = 0) {
  for (unsigned spins = 0;;) {
    const unsigned v = __hip_atomic_load(c.ctl + line * 32, RLX_AGENT);
    if (__all(v >= want)) break;
    __builtin_amdgcn_s_sleep(2);
    for (int i = nap; i > 0; --i) __builtin_amdgcn_s_sleep(4);
    if (spin_fail(spins, c.ctl, code)) break;
  }
  __builtin_amdgcn_fence(__ATOMIC_ACQUIRE, "wavefront");
}
DEV void set_line(const Ctx& c, int line, unsigned v) { __hip_atomic_store(c.ctl + line * 32, v, RLX_AGENT); }
DEV void ready_wait(const Ctx& c) {
  const unsigned long long e = __hip_atomic_load((gu64*)(c.ctl + L_EPOCH * 32), RLX_AGENT) + 1ull;
  for (unsigned spins = 0; spins < 4u * SPIN_CAP; ++spins) {
    if (__all(__hip_atomic_load((gu64*)(c.ctl + L_READY * 32), RLX_AGENT) == e)) break;
    __builtin_amdgcn_s_sleep(4);
  }
  __builtin_amdgcn_fence(__ATOMIC_ACQUIRE, "wavefront");
}
DEV void arrive_now(const Ctx& c) {
  if (c.tid == 0) __hip_atomic_fetch_add(c.ctl + (L_START + (blockIdx.x & 7)) * 32, 1u, RLX_AGENT);
  CONV();
}

template <int NT, class F>
DEV void load_wC(const Ctx& c, v8u (&w)[NT][10], const u16* s0, const u16* s1, int ld, int split, int tile0, F hook) {
  const int q = c.lane >> 4, qq = (c.lane >> 2) & 3, p = c.lane & 3;
  const u16* src[6];
#pragma unroll
  for (int i = 0; i < 6; ++i) {
    const int byte = (c.wave * 6 + i) * 1024 + c.lane * 16, lr = byte / 384, cp = (byte % 384) >> 4;
    const int cs = cp ^ (((lr >> 2) & 3) << 1);
    src[i] = (cs < split ? s0 + cs * 8 : s1 + (cs - split) * 8) + (size_t)lr * ld;
  }
  auto issue = [&](int ch) {
#pragma unroll
    for (int i = 0; i < 6; ++i) glds16(src[i] + (size_t)ch * 128 * ld, c.l0 + (ch % 3) * 49152 + (c.wave * 6 + i) * 1024);
  };
  issue(0); issue(1); issue(2);
  const int ro = 16 * (q >> 1) + 4 * (q & 1) + (qq & 1) + 8 * (qq >> 1), sl = (q & 1) + 2 * (qq >> 1);
#pragma unroll
  for (int ch = 0; ch < 5; ++ch) {
    if (ch <= 2) VMWAIT(12); else if (ch == 3) VMWAIT(6); else VMWAIT(0);
    BAR();
    lbyte* base = c.L + (ch % 3) * 49152 + ro * 384 + (p & 1) * 8;
#pragma unroll
    for (int t = 0; t < NT; ++t)
#pragma unroll
      for (int hf = 0; hf < 2; ++hf) {
        lbyte* a = base + hf * 64 * 384 + (((2 * (tile0 + t) + (p >> 1)) ^ (sl << 1)) << 4);
        const v2u r0 = trd(a), r1 = trd(a + 2 * 384), r2 = trd(a + 32 * 384), r3 = trd(a + 34 * 384);
        w[t][2 * ch + hf] = (v8u){r0.x, r0.y, r1.x, r1.y, r2.x, r2.y, r3.x, r3.y};
      }
    BAR();
#pragma unroll
    for (int t = 0; t < NT; ++t) asm volatile("" : "+v"(w[t][2 * ch]), "+v"(w[t][2 * ch + 1]));
    if (ch + 3 < 5) issue(ch + 3);
    if (ch == 3) hook();
  }
}
DEV void load_wG(const Ctx& c, v4u (&w)[5][6], const u16* s0) {
  const int q = c.lane >> 4, qq = (c.lane >> 2) & 3, p = c.lane & 3;
  const int ro = 16 * (q >> 1) + 4 * (q & 1) + (qq & 1) + 8 * (qq >> 1);
  const u16* src[5];
#pragma unroll
  for (int i = 0; i < 5; ++i) { const int byte = i * 1024 + c.lane * 16; src[i] = s0 + (size_t)(byte / 160) * 640 + ((byte % 160) >> 4) * 8; }
  auto issue = [&](int ks) {
#pragma unroll
    for (int i = 0; i < 5; ++i) glds16(src[i] + (size_t)ks * 32 * 640, c.l0 + c.wave * 15360 + (ks % 3) * 5120 + i * 1024);
  };
  issue(0); issue(1); issue(2);
#pragma unroll
  for (int ks = 0; ks < 6; ++ks) {
    if (ks <= 3) VMWAIT(10); else if (ks == 4) VMWAIT(5); else VMWAIT(0);
#pragma unroll
    for (int t = 0; t < 5; ++t) {
      lbyte* a = c.L + c.wave * 15360 + (ks % 3) * 5120 + ro * 160 + t * 32 + p * 8;
      const v2u r0 = trd(a), r1 = trd(a + 2 * 160);
      w[t][ks] = (v4u){r0.x, r0.y, r1.x, r1.y};
    }
    asm volatile("s_waitcnt lgkmcnt(0)" ::: "memory");
    if (ks + 3 < 6) issue(ks + 3);
  }
  BAR();
}
template <int NZ>
DEV void load_wZ(const Ctx& c, v8u (&w)[NZ][3], const u16* s0, int tile0) {
  const int q = c.lane >> 4, qq = (c.lane >> 2) & 3, p = c.lane & 3;
#pragma unroll
  for (int ch = 0; ch < 3; ++ch)
#pragma unroll
    for (int i = 0; i < 5; ++i) {
      const int byte = (c.wave * 5 + i) * 1024 + c.lane * 16, lr = byte / 640, cp = (byte % 640) >> 4;
      const int s = (lr >> 2) & 3;
      glds16(s0 + (size_t)(ch * 64 + lr) * 640 + (cp ^ (s << 1)) * 8, c.l0 + ch * 40960 + (c.wave * 5 + i) * 1024);
    }
  VMWAIT(0);
  BAR();
  const int ro = 16 * (q >> 1) + 4 * (q & 1) + (qq & 1) + 8 * (qq >> 1), sl = (q & 1) + 2 * (qq >> 1);
#pragma unroll
  for (int ch = 0; ch < 3; ++ch)
#pragma unroll
    for (int t = 0; t < NZ; ++t) {
      const int tile = tile0 + t;
      lbyte* a = c.L + ch * 40960 + ro * 640 + (p & 1) * 8 + (((2 * tile + (p >> 1)) ^ (sl << 1)) << 4);
      const v2u r0 = trd(a), r1 = trd(a + 2 * 640), r2 = trd(a + 32 * 640), r3 = trd(a + 34 * 640);
      w[t][ch] = (v8u){r0.x, r0.y, r1.x, r1.y, r2.x, r2.y, r3.x, r3.y};
    }
  BAR();
#pragma unroll
  for (int t = 0; t < NZ; ++t) asm volatile("" : "+v"(w[t][0]), "+v"(w[t][1]), "+v"(w[t][2]));
}

typedef float f32x2 __attribute__((ext_vector_type(2)));
DEV void ln_row(unsigned (&d)[5]) {
  f32x2 v[5];
#pragma unroll
  for (int i = 0; i < 5; ++i) v[i] = (f32x2){bflo(d[i]), bfhi(d[i])};
  const f32x2 s2 = (v[0] + v[1]) + (v[2] + v[3]) + v[4];
  const float mean = wave_sum(s2.x + s2.y) * (1.0f / 640.0f);
  f32x2 q2 = {0.f, 0.f};
#pragma unroll
  for (int i = 0; i < 5; ++i) { const f32x2 e = v[i] - mean; q2 += e * e; }
  const float var = rbf(wave_sum(q2.x + q2.y) * (1.0f / 640.0f));
  const float m = rbf(mean), sd = rbf(sqrtf(rbf(var + 1e-5f)));
  const float isd = 1.0f / sd;
#pragma unroll
  for (int i = 0; i < 5; ++i) {
    const f32x2 e = v[i] - m;
    const unsigned t = pk2(e.x, e.y);
    d[i] = pk2(bflo(t) * isd, bfhi(t) * isd);
  }
}
DEV void phase_ln(const Ctx& c, const Params& p, int seg) {
  const unsigned* src = (const unsigned*)(p.x + (size_t)(32 * seg + 4 * c.wave) * 640) + c.lane;
  unsigned d[4][5];
#pragma unroll
  for (int r = 0; r < 4; ++r)
#pragma unroll
    for (int i = 0; i < 5; ++i) d[r][i] = src[r * 320 + i * 64];
  lbyte* im = c.L + (c.wave >> 2) * 21760 + ((c.lane >> 3) * 2 + ((c.lane >> 1) & 1)) * 272 + (c.wave & 3) * 64 + (2 * (c.lane & 1) + ((c.lane >> 2) & 1)) * 4;
#pragma unroll 1
  for (int pass = 0; pass < 2; ++pass) {
#pragma unroll
    for (int r = 0; r < 4; ++r) {
      ln_row(d[r]);
#pragma unroll
      for (int i = 0; i < 5; ++i) *(LAS unsigned*)(im + r * 16 + i * 16 * 272) = d[r][i];
    }
    BAR();
#pragma unroll
    for (int i = 0; i < 5; ++i) st16(c.rs, (pass ? OFF_Y : OFF_XN) + seg * 40960 + i * 8192 + c.tid * 16, lds128(c.L + (i * 32 + (c.tid >> 4)) * 272 + (c.tid & 15) * 16));
    VMWAIT(0);
    BAR();
    if (pass == 0) { if (c.wave == 0) ready_wait(c); arrive_now(c); }
    else { if (c.tid == 0) __hip_atomic_fetch_add(c.ctl + L_YCNT * 32, 1u, RLX_AGENT); CONV(); }
  }
}
DEV void start_wait(const Ctx& c, bool acq, bool needy = false) {
  if (c.wave == 0) {
    if (!needy) ready_wait(c);
    wait_lines(c, L_START + (c.lane & 7), 12u, 0x10);
    if (needy) wait_lines(c, L_YCNT, 64u, 0x11);
    if (acq) { __builtin_amdgcn_fence(__ATOMIC_ACQUIRE, "agent"); VMWAIT(0); }
  }
  BAR();
}

template <int NT, int PF, int K0, int K1, class F>
DEV void project(const v8u (&w)[NT][10], lbyte* in, f32x4 (&acc)[NT][2], F side) {
  if (K0 == 0) {
#pragma unroll
    for (int t = 0; t < NT; ++t) { acc[t][0] = (f32x4){0.f, 0.f, 0.f, 0.f}; acc[t][1] = acc[t][0]; }
  }
  v4u x[PF + 1][2];
#pragma unroll
  for (int ks = K0; ks < K0 + PF; ++ks) { x[ks % (PF + 1)][0] = lds128(in + ks * 1024); x[ks % (PF + 1)][1] = lds128(in + 20480 + ks * 1024); }
#pragma unroll
  for (int ks = K0; ks < K1; ++ks) {
    if (ks + PF < K1) { x[(ks + PF) % (PF + 1)][0] = lds128(in + (ks + PF) * 1024); x[(ks + PF) % (PF + 1)][1] = lds128(in + 20480 + (ks + PF) * 1024); }
    __builtin_amdgcn_sched_barrier(0);
#pragma unroll
    for (int t = 0; t < NT; ++t) {
      const v4u wf = (ks & 1) ? hi4(w[t][ks >> 1]) : lo4(w[t][ks >> 1]);
      acc[t][0] = mfma(wf, x[ks % (PF + 1)][0], acc[t][0]);
      acc[t][1] = mfma(wf, x[ks % (PF + 1)][1], acc[t][1]);
    }
    if (NT == 1 && K0 == 0) { if (K1 > 10) { if (ks >= 1 && ks <= 10) side(ks - 1); } else if (ks < 5) { side(2 * ks); side(2 * ks + 1); } }
    __builtin_amdgcn_sched_barrier(0);
  }
}
template <int NT, bool GELU>
DEV void stream_A(const Ctx& c, const Params& p, const u16* wsrc, int ld, int in_off, int in_stride, int cnt, int out_off, int out_stride, int line, const u16* bias) {
  v8u w[NT][10];
  const int tile0 = NT == 2 ? 2 * c.wave : 4 + c.wave;
  const unsigned char* gin = p.ws + in_off + (c.wave & 3) * 10240 + c.lane * 16;
  load_wC<NT>(c, w, wsrc, wsrc, ld, 24, tile0, [&]() {
    if (!GELU) start_wait(c, true);
    if (NT == 2) {
#pragma unroll
      for (int i = 0; i < 10; ++i) glds16(gin + i * 1024, c.l0 + 98304 + (c.wave & 3) * 10240 + i * 1024);
    }
  });
  const int n = c.lane & 15, rq = c.lane >> 4;
  float bs[NT][4];
  if (GELU) {
#pragma unroll
    for (int t = 0; t < NT; ++t) {
      const v2u bb = *(const v2u*)(bias + 16 * (tile0 + t) + 4 * rq);
      bs[t][0] = bflo(bb.x); bs[t][1] = bfhi(bb.x); bs[t][2] = bflo(bb.y); bs[t][3] = bfhi(bb.y);
    }
  }
  const int oo = out_off + (((c.lane >> 5) * 6 + (tile0 >> 1)) * 64 + (2 * (tile0 & 1) + (rq & 1)) * 16 + n) * 16;
  if (NT == 1) {
#pragma unroll
    for (int i = 0; i < 10; ++i) glds16(gin + in_stride + i * 1024, c.l0 + 16384 + (c.wave & 3) * 10240 + i * 1024);
  }
  if (NT == 1) __builtin_amdgcn_s_setprio(1);
  int slot = 2;
#pragma unroll 1
  for (int it = 0; it < cnt; ++it) {
    if (NT == 2) VMWAIT(2); else VMWAIT(11);
    BAR();
    if (!GELU && it >= 6 && (it < 16 || (it & 3) == 0) && c.tid == 0) set_line(c, line, (unsigned)it);
    CONV();
    if (!GELU && it == 7) start_wait(c, true, true);
    const int nslot = slot == 0 ? 2 : slot - 1;
    const bool more = it + 2 < cnt;
    const unsigned char* gn = gin + (size_t)(more ? it + 2 : it) * in_stride;
    f32x4 acc[NT][2];
    project<NT, 2, 0, 20>(w, c.L + 16384 + slot * 40960 + c.lane * 16, acc, [&](int i) { glds16(gn + i * 1024, c.l0 + 16384 + nslot * 40960 + (c.wave & 3) * 10240 + i * 1024); });
#pragma unroll
    for (int t = 0; t < NT; ++t) {
      if (GELU) {
#pragma unroll
        for (int rt = 0; rt < 2; ++rt)
#pragma unroll
          for (int r = 0; r < 4; ++r) {
            const float u = rbf(rbf(acc[t][rt][r]) + bs[t][r]);
            const float z = u * (1.0f + 0.044715f * u * u) * (-2.0f * 0.7978845608028654f * 1.4426950408889634f);
            acc[t][rt][r] = u * __builtin_amdgcn_rcpf(1.0f + __builtin_amdgcn_exp2f(z));
          }
      }
      auto s0 = __builtin_amdgcn_permlane32_swap(pk2(acc[t][0][0], acc[t][0][1]), pk2(acc[t][1][0], acc[t][1][1]), false, false);
      auto s1 = __builtin_amdgcn_permlane32_swap(pk2(acc[t][0][2], acc[t][0][3]), pk2(acc[t][1][2], acc[t][1][3]), false, false);
      st16(c.rs, oo + it * out_stride + t * 512, (v4u){(unsigned)s0[0], (unsigned)s0[1], (unsigned)s1[0], (unsigned)s1[1]});
    }
    if (!GELU && it < 4) {
      VMWAIT(0);
      BAR();
      if (c.tid == 0) set_line(c, line, (unsigned)(it + 2));
      CONV();
    }
    slot = slot == 2 ? 0 : slot + 1;
  }
  __builtin_amdgcn_s_setprio(0);
  VMWAIT(0);
  BAR();
  if (c.tid == 0) set_line(c, line, (unsigned)(GELU ? cnt : cnt + 1));
  CONV();
}

DEV unsigned umax3(unsigned a, unsigned b, unsigned c) { return max(max(a, b), c); }
DEV unsigned mx8(v4u a, v4u b) { return umax3(umax3(a.x, a.y, a.z), umax3(a.w, b.x, b.y), max(b.z, b.w)); }
constexpr int OOB = 0x60000000;
DEV unsigned mx4(v4u a) { return max(umax3(a.x, a.y, a.z), a.w); }
DEV float vmax(float a, float b) { float r; asm("v_max_f32 %0, %1, %2" : "=v"(r) : "v"(a), "v"(b)); return r; }
DEV float vmax3(float a, float b, float c) { float r; asm("v_max3_f32 %0, %1, %2, %3" : "=v"(r) : "v"(a), "v"(b), "v"(c)); return r; }
constexpr int C_MEM = 0, C_IMG = 10240, C_RED = 13312, C_XI = 16384, C_XN = 98304;

DEV unsigned xcc_id() { return (unsigned)__builtin_amdgcn_s_getreg(20 | (3 << 11)) & 7u; }
DEV void side_vote(const Ctx& c) {
  unsigned best[2] = {~0u, ~0u};
  const int o = c.nr + NR_CAL + c.wave * 16384 + (blockIdx.x & 63) * 128;
#pragma unroll 1
  for (int i = 0; i < 8; ++i) {
    const unsigned t0 = (unsigned)__builtin_amdgcn_s_memtime();
    __builtin_amdgcn_raw_buffer_store_b32(t0, c.rs, o + (i & 1) * 8192, 0, 16);
    VMWAIT(0);
    const unsigned d = (unsigned)__builtin_amdgcn_s_memtime() - t0;
    if (i & 1) best[1] = min(best[1], d); else best[0] = min(best[0], d);
  }
  if (c.lane == 0) {
    __hip_atomic_fetch_add(c.ctl + L_VOTE * 32 + xcc_id(), best[1] < best[0] ? 1u : 0x10000u, RLX_AGENT);
    __hip_atomic_fetch_add(c.ctl + L_VOTE * 32 + 8, (best[1] < best[0]) == ((blockIdx.x & 4) == 0) ? 1u : 0x10000u, RLX_AGENT);
  }
  CONV();
}
DEV int side_get(const Ctx& c) {
  const unsigned v = __builtin_amdgcn_readfirstlane(__hip_atomic_load(c.ctl + L_VOTE * 32 + xcc_id(), RLX_AGENT));
  return (v & 0xFFFFu) > (v >> 16) ? 8192 : 0;
}
DEV int zm_half(const Ctx& c, int g) {
  const unsigned v = __builtin_amdgcn_readfirstlane(__hip_atomic_load(c.ctl + L_VOTE * 32 + 8, RLX_AGENT));
  return (((v & 0xFFFFu) > (v >> 16)) != (g != 0)) ? 8192 : 0;
}
struct Trk { int v, h1, h2; };
DEV void trk(Trk& t, int x, int up) {
  x = min(x, t.v + up);
  t.v += min(max(max(x, max(t.h1, t.h2)) - t.v, -2), up);
  t.h2 = t.h1; t.h1 = x;
}
DEV void sched_load(const Ctx& c, int s, unsigned& v0, unsigned& v1) {
  const int o = c.nr + (s & 3) * NR_SLOT + NR_STAMP;
  v0 = __builtin_amdgcn_raw_buffer_load_b32(c.rs, o + c.lane * 16, 0, 16);
  v1 = __builtin_amdgcn_raw_buffer_load_b32(c.rs, o + 1024 + (c.lane == 16 && s == 2 ? 16 : (c.lane & 15)) * 16, 0, 16);
}
DEV unsigned sched_eff(const Ctx& c, const Params& p, unsigned ia, int s, unsigned v0, unsigned v1, Trk& T) {
  if (__builtin_expect(!__all((v0 & 255u) == (unsigned)s && (v1 & 255u) == (unsigned)s), 0)) {
    int tries = 0;
    do {
      if (++tries > 200) return ia;
      __builtin_amdgcn_s_sleep(2);
      asm volatile("" ::: "memory");
      sched_load(c, s, v0, v1);
    } while (!__all((v0 & 255u) == (unsigned)s && (v1 & 255u) == (unsigned)s));
  }
  const unsigned now = rt32();
  if (s == 2) { T.v = min(max((int)((unsigned)__builtin_amdgcn_readlane(v1, 16) >> 8), 150), 600); if (c.lane == 16) v1 = v0; }
  unsigned m = min((now << 8) - v0, (now << 8) - v1) + 256u;
  m = min(m, (unsigned)__builtin_amdgcn_update_dpp(0, (int)m, DPP_XOR1, 0xf, 0xf, true));
  m = min(m, (unsigned)__builtin_amdgcn_update_dpp(0, (int)m, DPP_XOR2, 0xf, 0xf, true));
  m = min(m, (unsigned)__builtin_amdgcn_update_dpp(0, (int)m, DPP_ROR4, 0xf, 0xf, true));
  m = min(m, (unsigned)__builtin_amdgcn_update_dpp(0, (int)m, DPP_ROR8, 0xf, 0xf, true));
  m = min(min((unsigned)__builtin_amdgcn_readlane(m, 0), (unsigned)__builtin_amdgcn_readlane(m, 16)), min((unsigned)__builtin_amdgcn_readlane(m, 32), (unsigned)__builtin_amdgcn_readlane(m, 48)));
  const unsigned last = now - (m >> 8) + P_VIS;
  const int late = (int)(last - ia);
  if (s >= 3 && late < 12) trk(T, T.v + late + P_TM, 3);
  return late > 0 ? last + P_LATE + min(late >> 1, P_LATE_CAP) : ia;
}
DEV unsigned youngest(unsigned v) {
  const unsigned now = rt32();
  unsigned m = (now << 8) - v + 256u;
  m = min(m, (unsigned)__builtin_amdgcn_update_dpp(0, (int)m, DPP_XOR1, 0xf, 0xf, true));
  m = min(m, (unsigned)__builtin_amdgcn_update_dpp(0, (int)m, DPP_XOR2, 0xf, 0xf, true));
  m = min(m, (unsigned)__builtin_amdgcn_update_dpp(0, (int)m, DPP_ROR4, 0xf, 0xf, true));
  m = min(m, (unsigned)__builtin_amdgcn_update_dpp(0, (int)m, DPP_ROR8, 0xf, 0xf, true));
  m = min(min((unsigned)__builtin_amdgcn_readlane(m, 0), (unsigned)__builtin_amdgcn_readlane(m, 16)), min((unsigned)__builtin_amdgcn_readlane(m, 32), (unsigned)__builtin_amdgcn_readlane(m, 48)));
  return now - (m >> 8);
}
DEV void pace(unsigned t) {
  int rem;
  do { rem = (int)(t - rt32()); } while (rem > 9 && rem < 4000);
  if (rem >= 4000) rem = 0;
  for (rem = (rem * 3 - 4) >> 2; rem > 0; --rem) asm volatile("s_nop 0");
}

struct GatherA { int off, dst, off2, dst2; };
DEV int ga_dst(int pc) {
  const int r = pc >> 3, row = 2 * (r & 3) + ((pc >> 2) & 1), c8 = (r >> 2) * 4 + (pc & 3);
  return C_MEM + (c8 >> 3) * 1024 + (((c8 >> 1) & 3) * 16 + (c8 & 1) * 8 + row) * 16;
}
DEV void gatherA_setup(const Ctx& c, GatherA& g, int side) {
  const int pc = (c.wave & 3) * 128 + c.lane, pc2 = 512 + (c.wave & 3) * 32 + (c.lane & 31);
  g.off = c.nr + side + nr_mem(pc >> 3) + (pc & 7) * 16; g.dst = ga_dst(pc);
  g.off2 = c.lane < 32 ? c.nr + side + nr_mem(pc2 >> 3) + (pc2 & 7) * 16 : OOB; g.dst2 = c.lane < 32 ? ga_dst(pc2) : C_RED + c.lane * 16;
  asm volatile("" : "+v"(g.off), "+v"(g.dst), "+v"(g.off2), "+v"(g.dst2));
}
DEV void gatherA_first(const Ctx& c, const Params& p) {
#pragma unroll
  for (int i = 0; i < 2; ++i) {
    const int pc = i * 512 + c.tid;
    if (pc < 640) *(LAS v4u*)(c.L + ga_dst(pc)) = *(const v4u*)(p.mem0 + (2 * ((pc >> 3) & 3) + ((pc >> 2) & 1)) * 640 + ((pc >> 5) * 4 + (pc & 3)) * 8);
  }
}
DEV void gatherA(const Ctx& c, const GatherA& g, int s, int& dl, int cap) {
  const int base = (s & 3) * NR_SLOT;
  for (int i = dl; i > 0; --i) asm volatile("s_nop 0");
  v4u v0 = __builtin_amdgcn_raw_buffer_load_b128(c.rs, g.off, base, 16), v1 = __builtin_amdgcn_raw_buffer_load_b128(c.rs, g.off + 1024, base, 16);
  v4u v2 = __builtin_amdgcn_raw_buffer_load_b128(c.rs, g.off2, base, 16);
  unsigned spins = 0;
  *(LAS v4u*)(c.L + g.dst) = v0;
  *(LAS v4u*)(c.L + g.dst + 1024) = v1;
  *(LAS v4u*)(c.L + g.dst2) = v2;
  if (__builtin_expect(!__all(max(mx8(v0, v1), mx4(v2)) != SENT), 0)) {
    do {
      if (spin_fail(spins, c.ctl, 0x100 + s)) break;
      asm volatile("" ::: "memory");
      if (mx4(v0) == SENT) v0 = __builtin_amdgcn_raw_buffer_load_b128(c.rs, g.off, base, 16);
      if (mx4(v1) == SENT) v1 = __builtin_amdgcn_raw_buffer_load_b128(c.rs, g.off + 1024, base, 16);
      if (mx4(v2) == SENT) v2 = __builtin_amdgcn_raw_buffer_load_b128(c.rs, g.off2, base, 16);
      CONV();
    } while (!__all(max(mx8(v0, v1), mx4(v2)) != SENT));
    *(LAS v4u*)(c.L + g.dst) = v0;
    *(LAS v4u*)(c.L + g.dst + 1024) = v1;
    *(LAS v4u*)(c.L + g.dst2) = v2;
    if (spins == 1 && s > 4) dl = min(dl + 1, cap);
  }
}
template <int NT, int RG, class F>
DEV void stage1(const Ctx& c, const v8u (&w)[NT][10], int idx, lbyte* iw, F hook) {
  lbyte* in = c.L + C_MEM + c.lane * 16;
  f32x4 acc[NT];
#pragma unroll
  for (int t = 0; t < NT; ++t) acc[t] = (f32x4){0.f, 0.f, 0.f, 0.f};
  v4u a[RG];
#pragma unroll
  for (int ks = 0; ks < RG; ++ks) a[ks] = lds128(in + ks * 1024);
  __builtin_amdgcn_sched_barrier(0);
  hook();
  __builtin_amdgcn_sched_barrier(0);
#pragma unroll
  for (int ks = 0; ks < 10; ++ks) {
#pragma unroll
    for (int t = 0; t < NT; ++t) acc[t] = smfma(a[ks % RG], w[t][ks], acc[t], idx);
    if (ks + RG < 10) { a[ks % RG] = lds128(in + (ks + RG) * 1024); __builtin_amdgcn_sched_barrier(0); }
  }
  if (NT == 2) *(LAS v2u*)iw = (v2u){pk2(sw32_add(acc[0][0], acc[NT - 1][0]), sw32_add(acc[0][1], acc[NT - 1][1])), pk2(sw32_add(acc[0][2], acc[NT - 1][2]), sw32_add(acc[0][3], acc[NT - 1][3]))};
  else *(LAS unsigned*)iw = pk2(sw32_add(acc[0][0], acc[0][2]), sw32_add(acc[0][1], acc[0][3]));
  BAR();
}
template <int NT>
DEV void role_S(const Ctx& c, const Params& p, int h, int g) {
  v8u w[NT][10];
  const int tile0 = NT == 2 ? 2 * c.wave : 4 + c.wave;
  const unsigned char* gx = p.ws + OFF_XN + (c.wave & 3) * 10240 + c.lane * 16;
  load_wC<NT>(c, w, p.wq + h * 192 + g * 96, p.wk + h * 192 + g * 96, 6144, 12, tile0, [&]() {
    start_wait(c, true);
    if (NT == 2) {
#pragma unroll
      for (int i = 0; i < 10; ++i) glds16(gx + i * 1024, c.l0 + C_XN + (c.wave & 3) * 10240 + i * 1024);
    }
  });
  GatherA ga; int gdl = 0;
  const int near = side_get(c);
  if (NT == 2) gatherA_setup(c, ga, near);
  gatherA_first(c, p);
  const int n = c.lane & 15, rq = c.lane >> 4, qq = (c.lane >> 2) & 3, pp = c.lane & 3;
  const int idx = (n < 8) ? 0x4444 : 0xEEEE;
  const int sid = h * 2 + g;
  const int tl = NT == 2 ? tile0 + (c.lane >> 5) : tile0;
  lbyte* iw = c.L + C_IMG + (16 * (tl % 6) + n) * 32 + (tl / 6) * 16 + (rq & 1) * 8 + (NT == 2 ? 0 : (c.lane >> 5) * 4);
  lbyte* ir = c.L + C_IMG + (8 * rq + qq) * 32 + pp * 8;
  const int sox = n < 8 ? (n >> 1) * 128 + (rq >> 1) * 64 + (n & 1) * 32 + (rq & 1) * 16 : OOB, som = n < 8 && rq >= 2 ? 512 + (n >> 1) * 32 + (rq - 2) * 16 + (n & 1) * 8 : OOB;
  int xo[NT];
#pragma unroll
  for (int t = 0; t < NT; ++t) { const int T = tile0 + t, e = 16 * (T % 6) + 4 * rq; xo[t] = (T / 6) * 6144 + ((e >> 5) * 64 + ((e >> 3) & 3) * 16 + n) * 16 + ((e >> 2) & 1) * 8; }
  f32x4 xacc[NT][2];
  auto xput = [&](int sg) {
#pragma unroll
    for (int t = 0; t < NT; ++t)
#pragma unroll
      for (int rt = 0; rt < 2; ++rt) *(LAS v2u*)(c.L + C_XI + (sg & 1) * 12288 + xo[t] + rt * 3072) = (v2u){pk2(xacc[t][rt][0], xacc[t][rt][1]), pk2(xacc[t][rt][2], xacc[t][rt][3])};
  };
  auto xproj = [&](int sg, auto side) {
    project<NT, 2, 0, NT == 2 ? 20 : KSPLIT>(w, c.L + C_XN - (sg & 1) * 40960 + c.lane * 16, xacc, side);
    if (NT == 2) xput(sg);
  };
  xproj(0, [&](int i) { glds16(gx + 40960 + i * 1024, c.l0 + C_XN - 40960 + (c.wave & 3) * 10240 + i * 1024); });
  unsigned ia = rt32() - (1u << 20);
  Trk T = {250, 0, 0};
  for (int s = 0; s < NSEG; ++s) {
    const bool last = s == NSEG - 1;
    if (NT == 1 && c.wave == 6) pace(ia);
    __builtin_amdgcn_s_setprio(3);
    BAR();
    lbyte* img = c.L + C_XI + (s & 1) * 12288 + c.lane * 16;
    if (NT == 2) { if (s > 0) gatherA(c, ga, s, gdl, P_CAP_A); }
    else { project<NT, 3, KSPLIT, 20>(w, c.L + C_XN - (s & 1) * 40960 + c.lane * 16, xacc, [](int) {}); xput(s); }
    VMWAIT(0);
    BAR();
    v4u kx[2][3];
    unsigned sv0 = 0, sv1 = 0;
    stage1<NT, NT == 2 ? 6 : 10>(c, w, idx, iw, [&]() {
      if (NT == 1) sched_load(c, s, sv0, sv1);
      if (NT == 1 && c.wave == 5) {
#pragma unroll
        for (int kt = 0; kt < 2; ++kt)
#pragma unroll
          for (int ks = 0; ks < 3; ++ks) kx[kt][ks] = lds128(img + 6144 + kt * 3072 + ks * 1024);
      }
    });
    if (NT == 1) {
      v4u xf[3];
#pragma unroll
      for (int ks = 0; ks < 3; ++ks) { const v2u r0 = trd(ir + ks * 1024), r1 = trd(ir + ks * 1024 + 128); xf[ks] = (v4u){r0.x, r0.y, r1.x, r1.y}; }
      const int base = c.nr + (s & 3) * NR_SLOT + nr_sc(h) + g * 640;
      const int base2 = c.nr + ((s + 2) & 3) * NR_SLOT + nr_sc(h) + g * 640;
      if (__builtin_expect(c.wave == 5 && !last, 1)) {
        f32x4 c0 = {0.f, 0.f, 0.f, 0.f}, c1 = c0;
#pragma unroll
        for (int ks = 0; ks < 3; ++ks) { c0 = mfma(kx[0][ks], xf[ks], c0); c1 = mfma(kx[1][ks], xf[ks], c1); __builtin_amdgcn_sched_barrier(0); }
        const auto e0 = __builtin_amdgcn_permlane16_swap(pk2(c0[0], c0[1]), pk2(c0[2], c0[3]), false, false);
        const auto e1 = __builtin_amdgcn_permlane16_swap(pk2(c1[0], c1[1]), pk2(c1[2], c1[3]), false, false);
        const v4u d = {nosent(e0[0]), nosent(e1[0]), nosent(e0[1]), nosent(e1[1])};
        __builtin_amdgcn_raw_buffer_store_b128(d, c.rs, sox, base + (near ^ 8192), 16); __builtin_amdgcn_raw_buffer_store_b128(d, c.rs, sox, base + near, 16);
        const unsigned now = rt32();
        if (c.lane < 40) { st16(c.rs, base2 + c.lane * 16, (v4u){SENT, SENT, SENT, SENT}); st16(c.rs, base2 + 8192 + c.lane * 16, (v4u){SENT, SENT, SENT, SENT}); }
        if (c.lane == 0) __builtin_amdgcn_raw_buffer_store_b32((now << 8) | (unsigned)(s + 1), c.rs, c.nr + (s & 3) * NR_SLOT + NR_STAMP + 2048 + sid * 16, 0, 16);
        CONV();
      } else if (c.wave == 6 && !last) {
        f32x4 cm = {0.f, 0.f, 0.f, 0.f};
#pragma unroll
        for (int ks = 0; ks < 3; ++ks) cm = mfma(xf[ks], xf[ks], cm);
        const v2u dm = {nosent(pk2(cm[0], cm[1])), nosent(pk2(cm[2], cm[3]))};
        __builtin_amdgcn_raw_buffer_store_b64(dm, c.rs, som, base + (near ^ 8192), 16); __builtin_amdgcn_raw_buffer_store_b64(dm, c.rs, som, base + near, 16);
        __builtin_amdgcn_s_setprio(0);
        if (s >= 1) { ia = sched_eff(c, p, ia, s, sv0, sv1, T); ia += T.v; }
      } else if (c.wave == 5 || c.wave == 6) {
      } else {
        __builtin_amdgcn_s_setprio(0);
        const int rt = c.wave == 7;
        f32x4 c0 = {0.f, 0.f, 0.f, 0.f}, c1 = c0, cm = c0;
#pragma unroll
        for (int ks = 0; ks < 3; ++ks) {
          const v4u qf = lds128(img + rt * 3072 + ks * 1024);
          c0 = mfma(lds128(img + 6144 + ks * 1024), qf, c0); c1 = mfma(lds128(img + 9216 + ks * 1024), qf, c1); cm = mfma(xf[ks], qf, cm);
        }
        const int so = OFF_SX + ((s * 64 + sid) * 2 + rt) * 1280;
        st16(c.rs, so + c.lane * 16, (v4u){pk2(c0[0], c0[1]), pk2(c0[2], c0[3]), pk2(c1[0], c1[1]), pk2(c1[2], c1[3])});
        if (rq >= 2) st8(c.rs, so + 1024 + (c.lane - 32) * 8, (v2u){pk2(cm[0], cm[1]), pk2(cm[2], cm[3])});
        if (s >= 1 && c.tid == 448) set_line(c, L_PS + sid, (unsigned)s);
        CONV();
      }
    }
    if (NT == 2) { __builtin_amdgcn_s_setprio(0); if (c.wave == 1) __builtin_amdgcn_s_sleep(4); else if (c.wave == 2) __builtin_amdgcn_s_sleep(3); else __builtin_amdgcn_s_sleep(1); } else __builtin_amdgcn_s_setprio(1);
    if (!last) {
      const unsigned char* gn = gx + (size_t)min(s + 2, NSEG - 1) * 40960;
      xproj(s + 1,
                    [&](int i) { glds16(gn + i * 1024, c.l0 + C_XN - (s & 1) * 40960 + (c.wave & 3) * 10240 + i * 1024); });
    }
  }
  VMWAIT(0);
  BAR();
  if (c.tid == 0) set_line(c, L_PS + sid, (unsigned)NSEG);
  CONV();
}

template <int NZ>
DEV void uv_project(const Ctx& c, const v8u (&wz)[NZ][3], lbyte* in, int go) {
  constexpr int NP = NZ == 4 ? 2 : 1, TP = NZ / NP;
  v4u x[2][2];
  x[0][0] = lds128(in); x[0][1] = lds128(in + 6144);
#pragma unroll
  for (int ps = 0; ps < NP; ++ps) {
    f32x4 acc[TP][2];
#pragma unroll
    for (int t = 0; t < TP; ++t) { acc[t][0] = (f32x4){0.f, 0.f, 0.f, 0.f}; acc[t][1] = acc[t][0]; }
#pragma unroll
    for (int ks = 0; ks < 6; ++ks) {
      if (ks < 5 || ps + 1 < NP) { x[(ks + 1) & 1][0] = lds128(in + ((ks + 1) % 6) * 1024); x[(ks + 1) & 1][1] = lds128(in + 6144 + ((ks + 1) % 6) * 1024); }
      __builtin_amdgcn_sched_barrier(0);
#pragma unroll
      for (int t = 0; t < TP; ++t) {
        const v4u wf = (ks & 1) ? hi4(wz[ps * TP + t][ks >> 1]) : lo4(wz[ps * TP + t][ks >> 1]);
        acc[t][0] = mfma(x[ks & 1][0], wf, acc[t][0]);
        acc[t][1] = mfma(x[ks & 1][1], wf, acc[t][1]);
      }
      __builtin_amdgcn_sched_barrier(0);
    }
#pragma unroll
    for (int t = 0; t < TP; ++t)
      st16(c.rs, go + (ps * TP + t) * 1024, (v4u){pk2(acc[t][0][0], acc[t][0][1]), pk2(acc[t][0][2], acc[t][0][3]), pk2(acc[t][1][0], acc[t][1][1]), pk2(acc[t][1][2], acc[t][1][3])});
  }
}
struct PIn { v4u a, b; v2u ma, mb; };
DEV void p_load(const Ctx& c, PIn& q, int s, int h, int rt) {
  const int so = OFF_SX + ((s * 64 + 2 * h) * 2 + rt) * 1280;
  q.a = ld16(c.rs, so + c.lane * 16); q.b = ld16(c.rs, so + 2560 + c.lane * 16);
  q.ma = ld8(c.rs, so + 1024 + (c.lane & 31) * 8); q.mb = ld8(c.rs, so + 2560 + 1024 + (c.lane & 31) * 8);
}
DEV void p_math(const Ctx& c, const PIn& q, int rt, v4u& px, v2u& pm) {
  const int n = c.lane & 15, rq = c.lane >> 4;
  float v[12];
#pragma unroll
  for (int j = 0; j < 4; ++j) { v[2 * j] = bflo(q.a[j]) + bflo(q.b[j]); v[2 * j + 1] = bfhi(q.a[j]) + bfhi(q.b[j]); }
#pragma unroll
  for (int j = 0; j < 2; ++j) { v[8 + 2 * j] = bflo(q.ma[j]) + bflo(q.mb[j]); v[9 + 2 * j] = bfhi(q.ma[j]) + bfhi(q.mb[j]); }
  const int row = 16 * rt + n;
#pragma unroll
  for (int j = 0; j < 8; ++j) if ((j >> 2) * 16 + 4 * rq + (j & 3) > row) v[j] = -INFINITY;
  float mx = vmax(vmax3(vmax3(v[0], v[1], v[2]), vmax3(v[3], v[4], v[5]), vmax3(v[6], v[7], v[8])), vmax3(v[9], v[10], v[11]));
  mx = x16_max(mx); mx = x32_max(mx);
  mx *= -SCL2;
  float l = 0.f, lm = 0.f;
#pragma unroll
  for (int j = 0; j < 12; ++j) { v[j] = __builtin_amdgcn_exp2f(__builtin_fmaf(v[j], SCL2, mx)); if (j < 8) l += v[j]; else lm += v[j]; }
  l = __builtin_fmaf(lm, 0.5f, l);
  l = sw16_add(l, l); l = sw32_add(l, l);
  const float il = __builtin_amdgcn_rcpf(l);
  px = (v4u){pk2(v[0] * il, v[1] * il), pk2(v[2] * il, v[3] * il), pk2(v[4] * il, v[5] * il), pk2(v[6] * il, v[7] * il)};
  pm = (v2u){pk2(v[8] * il, v[9] * il), pk2(v[10] * il, v[11] * il)};
}
DEV void p_unit(const Ctx& c, const PIn& q, int s, int h, int rt) {
  const int n = c.lane & 15, rq = c.lane >> 4;
  v4u px; v2u pm;
  p_math(c, q, rt, px, pm);
  st16(c.rs, OFF_P + ((s * 32 + h) * 2 + rt) * 1024 + c.lane * 16, px);
  if (rq < 2) st8(c.rs, OFF_PM + ((s * 8 + (h >> 2)) * 2 + rt) * 1024 + ((h & 3) * 16 + n) * 16 + rq * 8, pm);
  CONV();
}
DEV void uv_need(const Ctx& c, int h, int seg, unsigned& seen) {
  if (seen < (unsigned)(seg + 1)) {
    for (unsigned spins = 0;;) {
      seen = __builtin_amdgcn_readfirstlane(__hip_atomic_load(c.ctl + (L_PV + h) * 32, RLX_AGENT));
      if (seen >= (unsigned)(seg + 1)) break;
      __builtin_amdgcn_s_sleep(2);
      if (spin_fail(spins, c.ctl, 0x400 + seg)) break;
    }
  }
  __builtin_amdgcn_fence(__ATOMIC_ACQUIRE, "wavefront");
}
DEV void uv_fetch(const Ctx& c, int h, int seg, unsigned& seen, int lo = C_XI) {
  uv_need(c, h, seg, seen);
  const int o = OFF_V + (seg * 32 + h) * 12288 + (c.wave - 4) * 3072 + c.lane * 16;
  const v4u r0 = ld16(c.rs, o), r1 = ld16(c.rs, o + 1024), r2 = ld16(c.rs, o + 2048);
  lbyte* in = c.L + lo + (seg & 1) * 12288 + (c.wave - 4) * 3072 + c.lane * 16;
  *(LAS v4u*)(in) = r0; *(LAS v4u*)(in + 1024) = r1; *(LAS v4u*)(in + 2048) = r2;
}
template <int NT>
DEV void role_UV(const Ctx& c0, const Params& p, int h, int g) {
  Ctx c = c0;
  constexpr int NZ = NT == 2 ? 1 : 4;
  v8u w[NT][10], wz[NZ][3];
  const int tile0 = NT == 2 ? 2 * c.wave : 4 + c.wave;
  const int ztile0 = NT == 2 ? c.wave : 4 * (c.wave - 3);
  const int uid = h * 2 + g;
  const int uo = OFF_U + h * 40960 + (g * 20 + ztile0) * 1024;
  unsigned vseen = 0;
  load_wZ<NZ>(c, wz, p.wo + (size_t)h * 192 * 640 + g * 320, ztile0);
  load_wC<NT>(c, w, p.wv + h * 192, p.wv, 6144, 24, tile0, [&]() {
    start_wait(c, true);
    if (NT == 2) {
#pragma unroll
      for (int i = 0; i < 10; ++i) glds16(p.ws + OFF_XN + c.wave * 10240 + c.lane * 16 + i * 1024, c.l0 + 98304 + c.wave * 10240 + i * 1024);
    }
  });
  asm volatile("" : "+v"(c.lane), "+v"(c.tid));
  {
    f32x4 acc[NT][2];
    project<NT, 2, 0, 20>(w, c.L + 98304 + c.lane * 16, acc, [](int) {});
#pragma unroll
    for (int t = 0; t < NT; ++t) {
      auto s0 = __builtin_amdgcn_permlane32_swap(pk2(acc[t][0][0], acc[t][0][1]), pk2(acc[t][1][0], acc[t][1][1]), false, false);
      auto s1 = __builtin_amdgcn_permlane32_swap(pk2(acc[t][0][2], acc[t][0][3]), pk2(acc[t][1][2], acc[t][1][3]), false, false);
      *(LAS v4u*)(c.L + C_XI + (((c.lane >> 5) * 6 + (tile0 >> 1)) * 64 + (2 * (tile0 & 1) + ((c.lane >> 4) & 1)) * 16 + (c.lane & 15)) * 16 + t * 512) = (v4u){(unsigned)s0[0], (unsigned)s0[1], (unsigned)s1[0], (unsigned)s1[1]};
    }
    BAR();
    uv_project<NZ>(c, wz, c.L + C_XI + c.lane * 16, uo + c.lane * 16);
    if (NT == 1) uv_fetch(c, h, 1, vseen);
    VMWAIT(0);
    BAR();
    if (c.tid == 0) set_line(c, L_PO + uid, 1u);
    CONV();
  }
  asm volatile("" : "+v"(c.lane), "+v"(c.tid));
  GatherA ga; int gdl = 0;
  if (NT == 2) gatherA_setup(c, ga, side_get(c));
  gatherA_first(c, p);
  const int n = c.lane & 15, rq = c.lane >> 4, qq = (c.lane >> 2) & 3, pp = c.lane & 3;
  const int idx = (n < 8) ? 0x4444 : 0xEEEE;
  const int tl = NT == 2 ? tile0 + (c.lane >> 5) : tile0;
  lbyte* iw = c.L + C_IMG + (16 * tl + n) * 16 + (rq & 1) * 8 + (NT == 2 ? 0 : (c.lane >> 5) * 4);
  lbyte* ir = c.L + C_IMG + (16 * rq + 8 * (pp >> 1) + qq) * 16 + (pp & 1) * 8;
  const int zo = (h * 640 + g * 320 + ztile0 * 16) * 16 + (NZ == 4 ? ((2 * (rq & 1) + (c.lane >> 5)) * 16 + n) * 16 : n * 16 + (rq & 1) * 8);
  const int zr = c.nr + nr_zm(h, (NZ == 4 ? (ztile0 + 2 * (rq & 1) + (c.lane >> 5)) : ztile0) * 16 + n, zm_half(c, g)) + (NZ == 4 ? 0 : (rq & 1) * 8);
  const int vo = OFF_V + h * 12288 + (c.wave & 3) * 3072 + c.lane * 16;
  int un = 1;
  unsigned ia = rt32() - (1u << 20);
  Trk T = {250, 0, 0};
  for (int s = 0; s < NSEG; ++s) {
    if (NT == 2 && c.wave == 3) pace(ia);
    __builtin_amdgcn_s_setprio(3);
    BAR();
    if (NT == 2 && s > 0) gatherA(c, ga, s, gdl, P_CAP_A);
    VMWAIT(0);
    BAR();
    unsigned sv0 = 0, sv1 = 0, pv = 0;
    v4u vr0, vr1, vr2;
    const bool vok = s > 0 && un + 1 < NSEG && vseen > (unsigned)(un + 1);
    stage1<NT, 5>(c, w, idx, iw, [&]() {
      if (NT == 1) {
        pv = __hip_atomic_load(c.ctl + (vseen < (unsigned)min(un + 5, NSEG) ? L_PV + h : L_ZERO) * 32, RLX_AGENT);
        const int o = vo + (vok ? (un + 1) * (32 * 12288) : OOB);
        vr0 = ld16(c.rs, o); vr1 = ld16(c.rs, o + 1024); vr2 = ld16(c.rs, o + 2048);
      }
    });
    f32x4 z[NZ];
#pragma unroll
    for (int t = 0; t < NZ; ++t) z[t] = (f32x4){0.f, 0.f, 0.f, 0.f};
    v4u va[3];
#pragma unroll
    for (int ks = 0; ks < 3; ++ks) { const v2u r0 = trd(ir + ks * 1024), r1 = trd(ir + ks * 1024 + 64); va[ks] = (v4u){r0.x, r0.y, r1.x, r1.y}; }
    __builtin_amdgcn_sched_barrier(0);
#pragma unroll
    for (int ks = 0; ks < 3; ++ks)
#pragma unroll
      for (int t = 0; t < NZ; ++t) z[t] = smfma(va[ks], wz[t][ks], z[t], idx);
    const int base = (s & 3) * ZM_SLOT + zr, base2 = ((s + 2) & 3) * ZM_SLOT + zr;
    if (NZ == 4) {
      const unsigned a0 = pk2(sw32_add(z[0][0], z[1 % NZ][0]), sw32_add(z[0][1], z[1 % NZ][1])), a1 = pk2(sw32_add(z[0][2], z[1 % NZ][2]), sw32_add(z[0][3], z[1 % NZ][3]));
      const unsigned b0 = pk2(sw32_add(z[2 % NZ][0], z[3 % NZ][0]), sw32_add(z[2 % NZ][1], z[3 % NZ][1])), b1 = pk2(sw32_add(z[2 % NZ][2], z[3 % NZ][2]), sw32_add(z[2 % NZ][3], z[3 % NZ][3]));
      auto s0 = __builtin_amdgcn_permlane16_swap(a0, b0, false, false);
      auto s1 = __builtin_amdgcn_permlane16_swap(a1, b1, false, false);
      const v4u d = {nosent(s0[0]), nosent(s1[0]), nosent(s0[1]), nosent(s1[1])};
      st16(c.rs, base, d);
      __builtin_amdgcn_s_setprio(0);
      st16(c.rs, base2, (v4u){SENT, SENT, SENT, SENT});
      st16(c.rs, OFF_ZMP + s * ZM_SLOT + zo, d);
      {
        lbyte* in = c.L + C_XI + (vok ? ((un + 1) & 1) * 12288 : 24576) + (c.wave & 3) * 3072 + c.lane * 16;
        *(LAS v4u*)(in) = vr0; *(LAS v4u*)(in + 1024) = vr1; *(LAS v4u*)(in + 2048) = vr2;
        vseen = max(vseen, (unsigned)__builtin_amdgcn_readfirstlane(pv));
        __builtin_amdgcn_fence(__ATOMIC_ACQUIRE, "wavefront");
      }
    } else {
      const v2u d = {nosent(pk2(sw32_add(z[0][0], z[0][0]), sw32_add(z[0][1], z[0][1]))), nosent(pk2(sw32_add(z[0][2], z[0][2]), sw32_add(z[0][3], z[0][3])))};
      if (c.lane < 32) st8(c.rs, base, d);
      CONV();
      __builtin_amdgcn_s_setprio(0);
      __builtin_amdgcn_s_sleep(5);
      if (c.lane < 32) { st8(c.rs, base2, (v2u){SENT, SENT}); st8(c.rs, OFF_ZMP + s * ZM_SLOT + zo, d); }
      if (s == NSEG - 1 && c.tid == 0) set_line(c, L_PUV + uid, (unsigned)s);
      CONV();
    }
    Ctx cb = c;
    asm volatile("" : "+v"(cb.lane));
    auto proj = [&]() { uv_project<NZ>(cb, wz, c.L + C_XI + (un & 1) * 12288 + cb.lane * 16, uo + cb.lane * 16 + un * (32 * 40960)); };
    if (s == 0) {
      proj();
      if (NT == 1) uv_fetch(cb, h, 2, vseen);
      VMWAIT(0);
      BAR();
      if (c.tid == 0) set_line(c, L_PO + uid, 2u);
      CONV();
      ++un;
    }
    if (un < NSEG) {
      proj();
      if (NT == 1 && !vok && un + 1 < NSEG) uv_fetch(cb, h, un + 1, vseen);
      ++un;
    }
    if (NT == 2 && c.wave == 3 && s >= 1) { sched_load(cb, s, sv0, sv1); ia = sched_eff(cb, p, ia, s, sv0, sv1, T); ia += T.v; }
  }
  VMWAIT(0);
  BAR();
  if (c.tid == 0) set_line(c, L_PUV + uid, (unsigned)NSEG);
  CONV();
}

DEV void role_R_pre(const Ctx& c, const Params& p, int ridx) {
  {
    const int jr = ridx % 10;
    if (jr < 8) phase_ln(c, p, jr * 8 + ridx / 10);
    else {
      if (c.wave < 2) { ready_wait(c); side_vote(c); }
      VMWAIT(0);
      BAR();
      arrive_now(c);
    }
  }
  start_wait(c, false);
}
DEV void role_Rc(const Ctx& c, const Params& p, int ridx) {
  const int side = side_get(c);
  const int rho = ridx & 3, cs = ridx >> 2, m = c.lane & 15, qa = c.lane >> 4, hp0 = m >> 2;
  const int hl = 8 * c.wave + 2 * hp0 + (qa >> 1);
  const int osx = c.nr + side + nr_sc(hl) + rho * 128 + (qa & 1) * 64 + (m & 3) * 16;
  const int osm = c.nr + side + nr_sc(hl) + 512 + rho * 32 + (qa & 1) * 16 + (m & 3) * 4;
  const int ozm = c.nr + nr_zm(8 * c.wave + 2 * qa, 32 * cs + m, zm_half(c, cs >= 10));
  const int ou = OFF_U + (8 * c.wave * 40 + 2 * cs) * 1024 + c.lane * 16;
  const int idx = (m & 1) ? 0xEEEE : 0x4444;
  unsigned mk[6];
#pragma unroll
  for (int k = 0; k < 4; ++k) { mk[k] = hp0 == k ? ~0u : 0u; asm volatile("" : "+v"(mk[k])); }
#pragma unroll
  for (int k = 0; k < 2; ++k) { mk[4 + k] = ((hp0 >> 1) == (qa & 1) && (hp0 & 1) == k) ? ~0u : 0u; asm volatile("" : "+v"(mk[4 + k])); }
  int d0 = 0, d2 = P_DZ;
  for (int s = 0; s < NSEG - 1; ++s) {
    if (s < 2) wait_lines(c, L_PO + (8 * c.wave + (c.lane & 7)) * 2 + (cs >= 10), (unsigned)(s + 1), 0x300 + s);
    v8u u[4][2];
#pragma unroll
    for (int hp = 0; hp < 4; ++hp) {
      if (hp == 2) { asm volatile("" ::: "memory"); __builtin_amdgcn_s_barrier(); asm volatile("" ::: "memory"); }
#pragma unroll
      for (int t = 0; t < 2; ++t) {
        const v4u a = ld16(c.rs, ou + s * (32 * 40960) + (2 * hp) * 40960 + t * 1024), b = ld16(c.rs, ou + s * (32 * 40960) + (2 * hp + 1) * 40960 + t * 1024);
        u[hp][t] = (v8u){a.x, a.y, a.z, a.w, b.x, b.y, b.z, b.w};
      }
    }
    const int base = (s & 3) * ZM_SLOT, nb = (s & 3) * NR_SLOT;
    v4u xa, xb, z[2][2]; unsigned ma, mb;
    f32x4 zero4 = {0.f, 0.f, 0.f, 0.f}, zero5 = zero4;
    asm volatile("" : "+v"(zero4), "+v"(zero5));
    BAR();
    auto sc_load = [&]() {
      xa = __builtin_amdgcn_raw_buffer_load_b128(c.rs, osx, nb, 16); xb = __builtin_amdgcn_raw_buffer_load_b128(c.rs, osx + 640, nb, 16);
      __builtin_amdgcn_sched_barrier(0);
      ma = __builtin_amdgcn_raw_buffer_load_b32(c.rs, osm, nb, 16); mb = __builtin_amdgcn_raw_buffer_load_b32(c.rs, osm + 640, nb, 16);
    };
    auto z_load = [&]() {
#pragma unroll
      for (int k = 0; k < 4; ++k) z[k >> 1][k & 1] = __builtin_amdgcn_raw_buffer_load_b128(c.rs, ozm + (k >> 1) * 256 + (k & 1) * 512, base, 16);
    };
    for (int i = d0; i > 0; --i) asm volatile("s_nop 0");
    xa = __builtin_amdgcn_raw_buffer_load_b128(c.rs, osx, nb, 16); xb = __builtin_amdgcn_raw_buffer_load_b128(c.rs, osx + 640, nb, 16);
    asm volatile("" ::: "memory");
    asm volatile("s_nop %0" :: "i"(8 * P_DM - 1));
    ma = __builtin_amdgcn_raw_buffer_load_b32(c.rs, osm, nb, 16); mb = __builtin_amdgcn_raw_buffer_load_b32(c.rs, osm + 640, nb, 16);
    asm volatile("" ::: "memory");
    for (int i = d2; i > 0; --i) asm volatile("s_nop 0");
    z_load();
    __builtin_amdgcn_sched_barrier(0);
    f32x2 sv[5];
    auto soft = [&]() -> float {
#pragma unroll
      for (int k = 0; k < 4; ++k) sv[k] = (f32x2){bflo(xa[k]), bfhi(xa[k])} + (f32x2){bflo(xb[k]), bfhi(xb[k])};
      float mx, mt;
      asm("v_max3_f32 %0, %2, %3, %4\n\tv_max3_f32 %1, %5, %6, %7\n\tv_max3_f32 %0, %0, %8, %9\n\tv_max_f32 %0, %0, %1"
          : "=&v"(mx), "=&v"(mt) : "v"(sv[0].x), "v"(sv[0].y), "v"(sv[1].x), "v"(sv[1].y), "v"(sv[2].x), "v"(sv[2].y), "v"(sv[3].x), "v"(sv[3].y));
      __builtin_amdgcn_sched_barrier(0);
      sv[4] = (f32x2){bflo(ma), bfhi(ma)} + (f32x2){bflo(mb), bfhi(mb)};
      asm("v_max3_f32 %0, %0, %1, %2\n\ts_nop 1\n\tv_max_f32_dpp %0, %0, %0 quad_perm:[1,0,3,2] row_mask:0xf bank_mask:0xf" : "+v"(mx) : "v"(sv[4].x), "v"(sv[4].y));
      { auto t = __builtin_amdgcn_permlane16_swap(__builtin_bit_cast(unsigned, mx), __builtin_bit_cast(unsigned, mx), false, false);
        mx = vmax(__builtin_bit_cast(float, (unsigned)t[0]), __builtin_bit_cast(float, (unsigned)t[1])) * SCL2; }
#pragma unroll
      for (int k = 0; k < 5; ++k) { sv[k] = sv[k] * SCL2 - mx; sv[k].x = __builtin_amdgcn_exp2f(sv[k].x); sv[k].y = __builtin_amdgcn_exp2f(sv[k].y); }
      const f32x2 l2 = ((sv[0] + sv[1]) + (sv[2] + sv[3])) + sv[4];
      float l = l2.x + l2.y;
      l += dpp<DPP_XOR1>(l); l = sw16_add(l, l);
      return __builtin_amdgcn_rcpf(l);
    };
    unsigned spins = 0;
    float il = soft();
    if (__builtin_expect(__any(il != il), 0)) {
      sc_load();
      while (!__all(umax3(mx8(xa, xb), ma, mb) != SENT)) {
        if (spin_fail(spins, c.ctl, 0x200 + s)) break;
        asm volatile("" ::: "memory");
        if (umax3(mx8(xa, xb), ma, mb) == SENT) sc_load();
        CONV();
      }
      if (spins == 0 && s > 4) d0 = min(d0 + 1, P_CAP_R);
      ++spins;
      if (max(mx8(z[0][0], z[0][1]), mx8(z[1][0], z[1][1])) == SENT) z_load();
      CONV();
      il = soft();
      VMWAIT(0);
    }
    unsigned px[5];
#pragma unroll
    for (int k = 0; k < 5; ++k) sv[k] *= il;
    __builtin_amdgcn_sched_barrier(0);
#pragma unroll
    for (int k = 0; k < 5; ++k) px[k] = pk2(sv[k].x, sv[k].y);
    v4u av[4];
#pragma unroll
    for (int hp = 0; hp < 4; ++hp) av[hp] = (v4u){px[0] & mk[hp], px[1] & mk[hp], px[2] & mk[hp], px[3] & mk[hp]};
    const auto e = __builtin_amdgcn_permlane16_swap(px[4], px[4], false, false);
    v4u am = {e[0] & mk[4], e[1] & mk[4], e[0] & mk[5], e[1] & mk[5]};
    unsigned zc = max(mx8(z[0][0], z[0][1]), mx8(z[1][0], z[1][1]));
#pragma unroll
    for (int hp = 0; hp < 4; ++hp) { zero4 = smfma(av[hp], u[hp][0], zero4, idx); zero5 = smfma(av[hp], u[hp][1], zero5, idx); }
    __builtin_amdgcn_sched_group_barrier(0x2, 11, 0);
#pragma unroll
    for (int k = 0; k < 4; ++k) { __builtin_amdgcn_sched_group_barrier(0x8, 1, 0); __builtin_amdgcn_sched_group_barrier(0x2, 3, 0); }
#pragma unroll
    for (int k = 0; k < 4; ++k) { __builtin_amdgcn_sched_group_barrier(0x8, 1, 0); __builtin_amdgcn_sched_group_barrier(0x2, 4, 0); }
    asm volatile("" : "+v"(am), "+v"(zc));
    auto finish = [&]() -> float {
      const f32x4 a0 = smfma(am, __builtin_shufflevector(z[0][0], z[0][1], 0, 1, 2, 3, 4, 5, 6, 7), zero4, idx);
      const f32x4 a1 = smfma(am, __builtin_shufflevector(z[1][0], z[1][1], 0, 1, 2, 3, 4, 5, 6, 7), zero5, idx);
      return sw16_add(sw32_add(a0[0] + a0[1], a0[2] + a0[3]), sw32_add(a1[0] + a1[1], a1[2] + a1[3]));
    };
    if (__builtin_expect(!__all(zc != SENT), 0)) {
      if (spins == 0 && s > 4) d2 = min(d2 + 2, P_DZ + 2 * P_CAP_R);
      do {
        if (spin_fail(spins, c.ctl, 0x280 + s)) break;
        asm volatile("" ::: "memory");
        if (max(mx8(z[0][0], z[0][1]), mx8(z[1][0], z[1][1])) == SENT) z_load();
        CONV();
      } while (!__all(max(mx8(z[0][0], z[0][1]), mx8(z[1][0], z[1][1])) != SENT));
      VMWAIT(0);
    }
    const float o = finish();
    *(LAS float*)(c.L + C_RED + c.wave * 256 + c.lane * 4) = o;
    BAR();
    __builtin_amdgcn_s_barrier();
    asm volatile("" ::: "memory");
  }
  BAR();
}
DEV void role_Rh(const Ctx& c, const Params& p, int ridx) {
  const int m = c.lane & 15, qa = c.lane >> 4;
  Trk a1 = {128, 0, 0}; bool ontime = false;
  unsigned ia = rt32() - (1u << 20);
  Trk T = {250, 0, 0};
  unsigned ck0 = 0, cr0 = 0;
  const int om = c.lane < 32 ? c.nr + nr_mem(ridx) + qa * 8192 + m * 8 : OOB;
  for (int s = 0; s < NSEG - 1; ++s) {
    if (c.wave == 7) {
      unsigned sv0, sv1;
      pace(ia + P_STAMP);
      sched_load(c, s, sv0, sv1);
      const unsigned sv2 = __builtin_amdgcn_raw_buffer_load_b32(c.rs, c.nr + ((s - 1) & 3) * NR_SLOT + NR_STAMP + 2048 + c.lane * 16, 0, 16);
      asm volatile("" ::: "memory");
      __builtin_amdgcn_s_barrier();
      if (s == 0) { ck0 = (unsigned)__builtin_amdgcn_s_memtime(); cr0 = rt32(); }
      if (s == 1) {
        const float tpc = (float)(rt32() - cr0) * __builtin_amdgcn_rcpf((float)((unsigned)__builtin_amdgcn_s_memtime() - ck0));
        a1.v = __builtin_amdgcn_readfirstlane((int)(tpc * 2160.0f)) + P_A0;
        if (ridx == 0 && c.lane == 0) __builtin_amdgcn_raw_buffer_store_b32(((unsigned)((int)(tpc * 4310.0f) + P_T0) << 8) | 2u, c.rs, c.nr + 2 * NR_SLOT + NR_STAMP + 80 * 16, 0, 16);
        CONV();
      }
      if (s >= 1) {
        if (ontime && __all((sv2 & 255u) == (unsigned)s)) trk(a1, (int)(youngest(sv2) - (ia - T.v)) + P_AM, 4);
        const unsigned ia0 = ia;
        ia = sched_eff(c, p, ia, s, sv0, sv1, T);
        ontime = ia == ia0;
        pace(ia + a1.v);
        ia += T.v;
      }
    } else {
      VMWAIT(0);
      __builtin_amdgcn_s_barrier();
    }
    BAR();
    BAR();
    if (__builtin_expect(c.wave == 4, 1)) {
      lbyte* rp = c.L + C_RED + m * 16;
      const f32x4 t = (*(LAS f32x4*)rp + *(LAS f32x4*)(rp + 256)) + (*(LAS f32x4*)(rp + 512) + *(LAS f32x4*)(rp + 768));
      const v2u dd = {nosent(pk2(t[0], t[1])), nosent(pk2(t[2], t[3]))};
      __builtin_amdgcn_raw_buffer_store_b64(dd, c.rs, om, ((s + 1) & 3) * NR_SLOT, 16);
      const unsigned now = rt32();
      __builtin_amdgcn_raw_buffer_store_b64((v2u){SENT, SENT}, c.rs, om, ((s + 3) & 3) * NR_SLOT, 16);
      if (c.lane == 0) __builtin_amdgcn_raw_buffer_store_b32((now << 8) | (unsigned)(s + 1), c.rs, c.nr + ((s + 1) & 3) * NR_SLOT + NR_STAMP + ridx * 16, 0, 16);
      CONV();
    }
    __builtin_amdgcn_s_barrier();
    asm volatile("" ::: "memory");
  }
  BAR();
}

DEV void red_put(const Ctx& c, const f32x4 (&acc)[2][5]) {
#pragma unroll
  for (int rt = 0; rt < 2; ++rt)
#pragma unroll
    for (int t = 0; t < 5; ++t) *(LAS f32x4*)(c.L + ((rt * 5 + t) * 8 + c.wave) * 1024 + c.lane * 16) = acc[rt][t];
}
DEV void red_get(const Ctx& c, f32x4 (&sv)[2]) {
#pragma unroll
  for (int k = 0; k < 2; ++k) {
    sv[k] = (f32x4){0.f, 0.f, 0.f, 0.f};
    if (k == 0 || c.wave < 2) {
#pragma unroll
      for (int hb = 0; hb < 2; ++hb) {
        f32x4 t[4];
#pragma unroll
        for (int j = 0; j < 4; ++j) t[j] = *(LAS f32x4*)(c.L + ((k * 8 + c.wave) * 8 + hb * 4 + j) * 1024 + c.lane * 16);
        __builtin_amdgcn_sched_barrier(0);
        sv[k] += (t[0] + t[1]) + (t[2] + t[3]);
      }
    }
  }
}
DEV void role_W(const Ctx& c, const Params& p, int cb, int par) {
  {
    const int id = cb * 2 + par;
    if (id == 0) {
#pragma unroll
      for (int i = 0; i < 6; ++i) st16(c.rs, i * 8192 + c.tid * 16, (v4u){0u, 0u, 0u, 0u});
      VMWAIT(0);
      BAR();
      if (c.tid == 0) __hip_atomic_store((gu64*)(c.ctl + L_READY * 32), __hip_atomic_load((gu64*)(c.ctl + L_EPOCH * 32), RLX_AGENT) + 1ull, RLX_AGENT);
      CONV();
    }
#pragma unroll 1
    for (int i = 0; i < 14; ++i) st16(c.rs, c.nr + (i < 4 ? id * 32768 + i * 8192 : NR_ZM + id * 81920 + (i - 4) * 8192) + c.tid * 16, (v4u){SENT, SENT, SENT, SENT});
    if (c.wave == 0) ready_wait(c);
    VMWAIT(0);
    BAR();
    arrive_now(c);
    const v4u* wsrc = (const v4u*)(par ? p.w2 : p.w1) + cb * 15360 + c.tid;
    v4u acc = {0u, 0u, 0u, 0u};
    __builtin_amdgcn_s_sleep(127); __builtin_amdgcn_s_sleep(127); __builtin_amdgcn_s_sleep(127);
#pragma unroll 6
    for (int i = 0; i < 30; ++i) acc ^= wsrc[i * 512];
    asm volatile("" :: "v"(acc));
  }
  const int n = c.lane & 15, rq = c.lane >> 4;
  const int hp = 4 * cb + (c.wave >> 1), rp = c.wave & 1;
  const int l = c.lane & 15;
  unsigned tw = rt32();
  v4u uf[5][5];
#pragma unroll
  for (int k = 0; k < 4; ++k)
#pragma unroll
    for (int t = 0; t < 5; ++t) uf[k][t] = (v4u){0u, 0u, 0u, 0u};
#pragma unroll 1
  for (int s = par; s < NW + 2; s += 2) {
    const int sa = min(s, NW - 2 + par), sp = max(s - 2, par);
    if (s < NW) pace(tw);
    wait_lines(c, l < 8 ? L_PS + 2 * hp + (l & 1) : L_PP + sp, l < 8 ? (unsigned)(s < NW ? s + 1 : s) : (s >= 2 ? 8u : 0u), 0x600 + s, s < NW ? P_NAP : 0);
    tw = rt32() + 350;
    VMWAIT(0);
    BAR();
    if (s >= 4 && c.tid == 0) set_line(c, L_SD + cb * 2 + par, (unsigned)(s - 3));
    CONV();
    PIn q;
    p_load(c, q, sa, hp, rp);
    v4u pf[5][2];
    const int no = s >= 2 ? 0 : OOB;
#pragma unroll
    for (int hh = 0; hh < 4; ++hh)
#pragma unroll
      for (int rt = 0; rt < 2; ++rt) pf[hh][rt] = ld16(c.rs, no + OFF_P + ((sp * 32 + 4 * c.wave + hh) * 2 + rt) * 1024 + c.lane * 16);
#pragma unroll
    for (int rt = 0; rt < 2; ++rt) pf[4][rt] = ld16(c.rs, no + OFF_PM + ((sp * 8 + c.wave) * 2 + rt) * 1024 + c.lane * 16);
#pragma unroll
    for (int t = 0; t < 5; ++t) uf[4][t] = ld16(c.rs, no + OFF_ZMP + sp * ZM_SLOT + ((4 * c.wave + rq) * 640 + 80 * cb + 16 * t + n) * 16);
    __builtin_amdgcn_sched_barrier(0);
    if (s < NW) p_unit(c, q, sa, hp, rp);
    f32x4 acc[2][5];
#pragma unroll
    for (int t = 0; t < 5; ++t) { acc[0][t] = (f32x4){0.f, 0.f, 0.f, 0.f}; acc[1][t] = acc[0][t]; }
#pragma unroll
    for (int k = 0; k < 5; ++k)
#pragma unroll
      for (int t = 0; t < 5; ++t) { acc[0][t] = mfma(uf[k][t], pf[k][0], acc[0][t]); acc[1][t] = mfma(uf[k][t], pf[k][1], acc[1][t]); }
    red_put(c, acc);
    VMWAIT(0);
    BAR();
    if (s < NW) {
      if (c.tid == 0) __hip_atomic_fetch_add(c.ctl + (L_PP + s) * 32, 1u, RLX_AGENT);
      CONV();
#pragma unroll
      for (int hh = 0; hh < 4; ++hh)
#pragma unroll
        for (int t = 0; t < 5; ++t) uf[hh][t] = ld16(c.rs, OFF_U + (s * 32 + 4 * c.wave + hh) * 40960 + (5 * cb + t) * 1024 + c.lane * 16);
    }
    f32x4 sv[2];
    red_get(c, sv);
    if (s >= 2) {
      const int so = OFF_G + ((sp * 8 + cb) * 10 + c.wave) * 512 + c.lane * 8;
      st8(c.rs, so, (v2u){pk2(sv[0][0], sv[0][1]), pk2(sv[0][2], sv[0][3])});
      if (c.wave < 2) st8(c.rs, so + 4096, (v2u){pk2(sv[1][0], sv[1][1]), pk2(sv[1][2], sv[1][3])});
    }
  }
  VMWAIT(0);
  BAR();
  if (c.tid == 0) set_line(c, L_SD + cb * 2 + par, (unsigned)NSEG);
  CONV();
}
DEV void role_GO(const Ctx& c0, const Params& p, int cb, int part) {
  Ctx c = c0;
  v4u w[5][6];
  load_wG(c, w, p.w2 + (size_t)c.wave * 192 * 640 + 80 * cb);
  wait_lines(c, L_PH + c.wave * 4 + part, 16u, 0x800);
  v4u hf[2][6];
  unsigned sdseen = 0;
  const int ho = OFF_H + c.wave * 12288 + c.lane * 16;
#pragma unroll
  for (int i = 0; i < 12; ++i) hf[i / 6][i % 6] = ld16(c.rs, ho + (NW + part) * (8 * 12288) + i * 1024);
  v2u bb[2];
#pragma unroll
  for (int k = 0; k < 2; ++k) bb[k] = *(const v2u*)(p.b2 + 80 * cb + 16 * (min(k * 8 + c.wave, 9) % 5) + 4 * (c.lane >> 4));
  const rsrc_t rx = __builtin_amdgcn_make_buffer_rsrc((void*)p.x, 0, 2048 * 640 * 2, 0x00020000);
#pragma unroll 1
  for (int it = 0; it < 16; ++it) {
    const int s = it == 0 ? NW + part : part + 4 * (it - 1);
    asm volatile("" : "+v"(c.lane));
    const int n = c.lane & 15, rq = c.lane >> 4;
    const int sn = part + 4 * min(it, 14);
    const int so = OFF_G + ((s * 8 + cb) * 10 + c.wave) * 512 + c.lane * 8;
    v2u xx[2], ss[2];
    auto s_wait = [&]() {
      for (unsigned spins = 0;;) {
        sdseen = __builtin_amdgcn_readfirstlane(__hip_atomic_load(c.ctl + (L_SD + cb * 2 + (part & 1)) * 32, RLX_AGENT));
        if (sdseen >= (unsigned)(s + 1)) break;
        if (s < NW - 4) __builtin_amdgcn_s_sleep(32); else __builtin_amdgcn_s_sleep(4);
        if (spin_fail(spins, c.ctl, 0x900 + s)) break;
      }
      __builtin_amdgcn_fence(__ATOMIC_ACQUIRE, "wavefront");
    };
    const bool have = it == 0 || sdseen >= (unsigned)(s + 1);
    f32x4 acc[2][5];
#pragma unroll
    for (int rt = 0; rt < 2; ++rt) {
      if (rt == 1) {
        BAR();
#pragma unroll
        for (int k = 0; k < 2; ++k) {
          const int u = k * 8 + c.wave;
          xx[k] = __builtin_amdgcn_raw_buffer_load_b64(rx, u < 10 ? ((32 * s + 16 * (u / 5) + n) * 640 + 80 * cb + 16 * (u % 5) + 4 * rq) * 2 : OOB, 0, 0);
          ss[k] = ld8(c.rs, u < 10 && have && it > 0 ? so + k * 4096 : OOB);
        }
      }
#pragma unroll
      for (int t = 0; t < 5; ++t) acc[rt][t] = (f32x4){0.f, 0.f, 0.f, 0.f};
#pragma unroll
      for (int ks = 0; ks < 6; ++ks) {
#pragma unroll
        for (int t = 0; t < 5; ++t) acc[rt][t] = mfma(w[t][ks], hf[rt][ks], acc[rt][t]);
        __builtin_amdgcn_sched_barrier(0);
        hf[rt][ks] = ld16(c.rs, ho + sn * (8 * 12288) + (rt * 6 + ks) * 1024);
        if (rt == 1 && ks < 5) *(LAS f32x4*)(c.L + (ks * 8 + c.wave) * 1024 + c.lane * 16) = acc[0][ks];
        __builtin_amdgcn_sched_barrier(0);
      }
    }
    f32x4 gv[2];
#pragma unroll
    for (int t = 0; t < 5; ++t) *(LAS f32x4*)(c.L + ((5 + t) * 8 + c.wave) * 1024 + c.lane * 16) = acc[1][t];
    BAR();
    red_get(c, gv);
#pragma unroll
    for (int k = 0; k < 2; ++k) {
      if (k == 0 || c.wave < 2) {
        const float b[4] = {bflo(bb[k].x), bfhi(bb[k].x), bflo(bb[k].y), bfhi(bb[k].y)};
#pragma unroll
        for (int j = 0; j < 4; ++j) {
          const float u2 = rbf(rbf(gv[k][j]) + b[j]);
          gv[k][j] = rbf(u2 * __builtin_amdgcn_rcpf(1.0f + __builtin_amdgcn_exp2f(-1.4426950408889634f * u2)));
        }
      }
    }
    if (!have) { s_wait(); ss[0] = ld8(c.rs, so); ss[1] = ld8(c.rs, c.wave < 2 ? so + 4096 : OOB); }
#pragma unroll
    for (int k = 0; k < 2; ++k) {
      if (k == 0 || c.wave < 2) {
        const int u = k * 8 + c.wave, col = 80 * cb + 16 * (u % 5) + 4 * rq, row = 32 * s + 16 * (u / 5) + n;
        const float xv[4] = {bflo(xx[k].x), bfhi(xx[k].x), bflo(xx[k].y), bfhi(xx[k].y)}, sf[4] = {bflo(ss[k].x), bfhi(ss[k].x), bflo(ss[k].y), bfhi(ss[k].y)};
        float o[4];
#pragma unroll
        for (int j = 0; j < 4; ++j) o[j] = xv[j] + rbf(sf[j] * gv[k][j]);
        if (it == 0) st8(c.rs, so + k * 4096, (v2u){pk2(gv[k][0], gv[k][1]), pk2(gv[k][2], gv[k][3])});
        else *(v2u*)(p.out + (size_t)row * 640 + col) = (v2u){pk2(o[0], o[1]), pk2(o[2], o[3])};
      }
    }
    if (it == 0) {
      VMWAIT(0);
      BAR();
      if (c.tid == 0) set_line(c, L_GT + cb * 4 + part, 1u);
      CONV();
    }
  }
}
DEV void tail_unit(const Ctx& c, const Params& p, int s, int cs, int rt) {
  const int n = c.lane & 15, rq = c.lane >> 4, l = c.lane & 15;
  const int tile = 2 * cs + (c.wave & 1), cb = tile / 5;
  v4u uf[4][2];
#pragma unroll
  for (int hh = 0; hh < 4; ++hh)
#pragma unroll
    for (int t = 0; t < 2; ++t) uf[hh][t] = ld16(c.rs, OFF_U + (s * 32 + 4 * c.wave + hh) * 40960 + (2 * cs + t) * 1024 + c.lane * 16);
  const int col = 16 * tile + 4 * rq, row = 32 * s + 16 * rt + n;
  const v2u xx = *(const v2u*)(p.x + (size_t)row * 640 + col);
  wait_lines(c, l < 8 ? L_PS + 8 * c.wave + l : (l < 12 ? L_PUV + (4 * c.wave + l - 8) * 2 + (cs >= 10) : L_GT + cb * 4 + (s & 3)),
             l < 12 ? (s >= NSEG - 2 ? (unsigned)(s + 1) : 0u) : 1u, 0xA00 + s);
  PIn q[4];
#pragma unroll
  for (int hh = 0; hh < 4; ++hh) p_load(c, q[hh], s, 4 * c.wave + hh, rt);
  v2u za[2][2];
#pragma unroll
  for (int t = 0; t < 2; ++t)
#pragma unroll
    for (int k = 0; k < 2; ++k) za[t][k] = ld8(c.rs, OFF_ZMP + s * ZM_SLOT + ((4 * c.wave + 2 * k + (rq >> 1)) * 640 + 32 * cs + 16 * t + n) * 16 + (rq & 1) * 8);
  const v2u gg = ld8(c.rs, OFF_G + ((s * 8 + cb) * 10 + rt * 5 + tile % 5) * 512 + c.lane * 8);
  f32x4 acc[2] = {{0.f, 0.f, 0.f, 0.f}, {0.f, 0.f, 0.f, 0.f}};
  v2u pm[4];
#pragma unroll
  for (int hh = 0; hh < 4; ++hh) {
    v4u px;
    p_math(c, q[hh], rt, px, pm[hh]);
    acc[0] = mfma(uf[hh][0], px, acc[0]); acc[1] = mfma(uf[hh][1], px, acc[1]);
  }
  const v2u b0 = rq < 2 ? pm[0] : pm[1], b1 = rq < 2 ? pm[2] : pm[3];
  const v4u bm = {b0.x, b0.y, b1.x, b1.y};
#pragma unroll
  for (int t = 0; t < 2; ++t) {
    acc[t] = mfma((v4u){za[t][0].x, za[t][0].y, za[t][1].x, za[t][1].y}, bm, acc[t]);
    *(LAS f32x4*)(c.L + (t * 8 + c.wave) * 1024 + c.lane * 16) = acc[t];
  }
  BAR();
  if (c.wave < 2) {
    f32x4 sv = {0.f, 0.f, 0.f, 0.f};
#pragma unroll
    for (int j = 0; j < 8; ++j) sv += *(LAS f32x4*)(c.L + (c.wave * 8 + j) * 1024 + c.lane * 16);
    const float xv[4] = {bflo(xx.x), bfhi(xx.x), bflo(xx.y), bfhi(xx.y)}, gt[4] = {bflo(gg.x), bfhi(gg.x), bflo(gg.y), bfhi(gg.y)};
    float o[4];
#pragma unroll
    for (int j = 0; j < 4; ++j) o[j] = xv[j] + rbf(rbf(sv[j]) * gt[j]);
    *(v2u*)(p.out + (size_t)row * 640 + col) = (v2u){pk2(o[0], o[1]), pk2(o[2], o[3])};
  }
}

__global__ void __launch_bounds__(512, 2) rk_kernel(Params p) {
  extern __shared__ __attribute__((aligned(16))) unsigned char lds[];
  Ctx c;
  c.rs = __builtin_amdgcn_make_buffer_rsrc(p.ws, 0, WS_END, 0x00020000);
  c.ctl = (gu32*)p.ws;
  c.L = (lbyte*)lds;
  c.l0 = (unsigned)(uintptr_t)c.L;
  c.tid = threadIdx.x; c.wave = __builtin_amdgcn_readfirstlane(threadIdx.x >> 6); c.lane = threadIdx.x & 63;
  c.nr = OFF_NR + (int)((0u - (unsigned)(uintptr_t)(p.ws + OFF_NR)) & 0x1FFFFFu);
  const int b = blockIdx.x, x = b & 7, j = b >> 3;
  const int ridx = x * 10 + (j - 16);
  if (j < 8) {
    if (c.wave < 4) role_S<2>(c, p, x + 8 * (j >> 1), j & 1); else role_S<1>(c, p, x + 8 * (j >> 1), j & 1);
  } else if (j < 16) {
    const int uh = (x & 3) * 8 + j - 8, ug = x >> 2;
    if (c.wave < 4) role_UV<2>(c, p, uh, ug); else role_UV<1>(c, p, uh, ug);
  } else if (j < 26) {
    role_R_pre(c, p, ridx);
    if (c.wave < 4) role_Rc(c, p, ridx); else role_Rh(c, p, ridx);
  } else if (j >= 30) {
    role_W(c, p, x, j - 30);
  } else {
    const int jj = j - 26, h = (x & 3) * 8 + (x >> 2) * 4 + jj;
    if (c.wave < 4) stream_A<2, false>(c, p, p.wv + h * 192, 6144, OFF_XN + 40960, 40960, NSEG - 1, OFF_V + (32 + h) * 12288, 32 * 12288, L_PV + h, nullptr);
    else stream_A<1, false>(c, p, p.wv + h * 192, 6144, OFF_XN + 40960, 40960, NSEG - 1, OFF_V + (32 + h) * 12288, 32 * 12288, L_PV + h, nullptr);
    { int tz = threadIdx.x; asm volatile("" : "+v"(tz)); c.tid = tz; c.lane = tz & 63; }
    if (c.wave < 4) stream_A<2, true>(c, p, p.w1 + x * 192, 1536, OFF_Y + jj * 40960, 4 * 40960, 16, OFF_H + (jj * 8 + x) * 12288, 32 * 12288, L_PH + x * 4 + jj, p.b1 + x * 192);
    else stream_A<1, true>(c, p, p.w1 + x * 192, 1536, OFF_Y + jj * 40960, 4 * 40960, 16, OFF_H + (jj * 8 + x) * 12288, 32 * 12288, L_PH + x * 4 + jj, p.b1 + x * 192);
    { int tz = threadIdx.x; asm volatile("" : "+v"(tz)); c.tid = tz; c.lane = tz & 63; }
    role_GO(c, p, (x & 1) * 4 + jj, x >> 1);
    if (b == 26 * 8 && c.tid == 0) __hip_atomic_fetch_add((gu64*)(c.ctl + L_EPOCH * 32), 1ull, RLX_AGENT);
  }
  if (j < 26 && (j >= 16 || (j & 7) < 5)) {
    { int tz = threadIdx.x; asm volatile("" : "+v"(tz)); c.tid = tz; c.lane = tz & 63; }
    __builtin_amdgcn_s_setprio(0);
    BAR();
    const int u = x * 5 + (j & 7);
    tail_unit(c, p, j >= 16 ? NW + ((ridx >> 1) & 1) : (j < 8 ? NSEG - 2 : NSEG - 1), j >= 16 ? ridx >> 2 : u >> 1, j >= 16 ? ridx & 1 : u & 1);
  }
}
}

extern "C" void kernel_launch(void* const* d_in, const int* in_sizes, int n_in, void* d_out, int out_size, void* d_ws, size_t ws_size,
                              hipStream_t stream) {
  using namespace rk;
  (void)in_sizes; (void)n_in; (void)out_size; (void)ws_size;
  (void)hipFuncSetAttribute((const void*)rk_kernel, hipFuncAttributeMaxDynamicSharedMemorySize, LDS_BYTES);
  Params p;
  p.x = (const u16*)d_in[0]; p.mem0 = (const u16*)d_in[1]; p.wq = (const u16*)d_in[2]; p.wk = (const u16*)d_in[3];
  p.wv = (const u16*)d_in[4]; p.wo = (const u16*)d_in[5]; p.w1 = (const u16*)d_in[8]; p.b1 = (const u16*)d_in[9];
  p.w2 = (const u16*)d_in[10]; p.b2 = (const u16*)d_in[11];
  p.out = (u16*)d_out; p.ws = (unsigned char*)d_ws;
  rk_kernel<<<256, 512, LDS_BYTES, stream>>>(p);
}
```
